```python
import torch

def _softmax(x, axis=-1):
    return torch.softmax(x, dim=axis)
def _gelu(x):
    return torch.nn.functional.gelu(x, approximate="tanh")
def _silu(x):
    return torch.nn.functional.silu(x)
def _sigmoid(x):
    return torch.sigmoid(x)
def _layernorm(x, axis=-1, eps=1e-5):
    m = x.mean(dim=axis, keepdim=True)
    v = x.var(dim=axis, keepdim=True, unbiased=False)
    return (x - m) / torch.sqrt(v + eps)
def _scatter_add(tgt, idx, src, axis=0):
    return tgt.clone().index_add_(axis, idx, src)
def _segment_sum(src, seg, n):
    out = torch.zeros((n,) + src.shape[1:], dtype=src.dtype)
    return out.index_add_(0, seg, src)
def _pad(x, pw):
    flat = tuple(v for pair in reversed(pw) for v in pair)
    return torch.nn.functional.pad(x, flat)

kernel_name = 'gated_mix__compressed_kv_cla__swiglu_ffn__decode'

def setup_inputs():

    _w = (lambda a: (lambda t: t.to(torch.bfloat16) if t.is_floating_point() else t)(torch.as_tensor(a)))
    def _A_setup():
        B, H, Sq, Sk, D, M = 1, 32, 1, 8192, 128, 512
        Dc = 4 * D
        return {
            "x": _w((lambda *s: torch.randn(*s, dtype=torch.bfloat16) * 0.05)(B, Sq, M)),
            "c_kv": _w((lambda *s: torch.randn(*s, dtype=torch.bfloat16) * 0.8)(B, Sk, Dc)),
            "w_q": _w((lambda *s: torch.randn(*s, dtype=torch.bfloat16) * 6.4)(M, H, D) * (M ** -0.5)),
            "w_uk": _w((lambda *s: torch.randn(*s, dtype=torch.bfloat16) * 6.4)(Dc, H, D) * (Dc ** -0.5)),
            "w_uv": _w((lambda *s: torch.randn(*s, dtype=torch.bfloat16) * 0.8)(Dc, H, D) * (Dc ** -0.5)),
            "w_o": _w((lambda *s: torch.randn(*s, dtype=torch.bfloat16) * 0.8)(H, D, M) * ((H * D) ** -0.5)),
        }
    _A_in = _A_setup()

    def _B_setup():
        B, S, D, F = 1, 1, 512, 1376
        return {
            "x": _w((lambda *s: torch.randn(*s, dtype=torch.bfloat16) * 0.05)(B, S, D)),
            "w_gate": _w((lambda *s: torch.randn(*s, dtype=torch.bfloat16) * 0.1)(D, F)),
            "w_up": _w((lambda *s: torch.randn(*s, dtype=torch.bfloat16) * 0.1)(D, F)),
            "w_down": _w((lambda *s: torch.randn(*s, dtype=torch.bfloat16) * 0.1)(F, D)),
        }
    _B_in = _B_setup()

    return {
        "A_x": _A_in["x"],
        "A_c_kv": _A_in["c_kv"],
        "A_w_q": _A_in["w_q"],
        "A_w_uk": _A_in["w_uk"],
        "A_w_uv": _A_in["w_uv"],
        "A_w_o": _A_in["w_o"],
        "B_x": _B_in["x"],
        "B_w_gate": _B_in["w_gate"],
        "B_w_up": _B_in["w_up"],
        "B_w_down": _B_in["w_down"],
        "gate_w": _w((lambda *s: torch.randn(*s, dtype=torch.bfloat16) * 0.05)(512) * 16.0),
    }

def reference(A_x, A_c_kv, A_w_q, A_w_uk, A_w_uv, A_w_o, B_x, B_w_gate, B_w_up, B_w_down, gate_w):
    def _A_ref(x, c_kv, w_q, w_uk, w_uv, w_o):
        B, Sq, M = x.shape[0], x.shape[1], x.shape[2]
        Sk = c_kv.shape[1]
        H, D = w_q.shape[1], w_q.shape[2]
        q = torch.einsum("bqm,mhd->bhqd", x, w_q)
        k = torch.einsum("bkc,chd->bhkd", c_kv, w_uk)
        v = torch.einsum("bkc,chd->bhkd", c_kv, w_uv)
        scores = torch.einsum("bhqd,bhkd->bhqk", q, k) * (D ** -0.5)
        off = Sk - Sq
        qi = torch.arange(Sq)[:, None]
        kj = torch.arange(Sk)[None, :]
        cmask = kj <= (qi + off)
        scores = torch.where(cmask[None, None], scores, -1e9)
        p = _softmax(scores, axis=-1)
        ctx = torch.einsum("bhqk,bhkd->bhqd", p, v)
        return torch.einsum("bhqd,hdm->bqm", ctx, w_o)

    def _B_ref(x, w_gate, w_up, w_down):
        g = _silu(x @ w_gate)
        u = x @ w_up
        return (g * u) @ w_down

    x = A_x
    g = _sigmoid((x * gate_w).sum(-1, keepdims=True))
    a = _A_ref(x, A_c_kv, A_w_q, A_w_uk, A_w_uv, A_w_o)
    b = _B_ref(x, B_w_gate, B_w_up, B_w_down)
    return g * a + (1 - g) * b
```

```cpp
#include <hip/hip_runtime.h>
#include <hip/hip_bf16.h>


typedef unsigned short u16;
typedef unsigned v4u __attribute__((ext_vector_type(4)));
typedef unsigned v2u __attribute__((ext_vector_type(2)));
typedef float f32x4 __attribute__((ext_vector_type(4)));
typedef short bf16x8 __attribute__((ext_vector_type(8)));
typedef unsigned short u16x4 __attribute__((ext_vector_type(4)));

constexpr int H = 32, D = 128, MD = 512, DC = 512, SK = 8192, FF = 1376;
constexpr int NWG = 256, NT = 512;
constexpr int HG = 2;
constexpr int HPG = H / HG;
constexpr int NR = NWG / HG;
constexpr int KPR = SK / NR;
static_assert(HPG == 16 && KPR == 64, "geometry");

constexpr size_t WS_QA = 0;
constexpr size_t WS_PART = 64 * 1024;
constexpr size_t PART_BYTES = (size_t)H * 8 * NR * 64 * 4;
constexpr size_t WS_ML = WS_PART + PART_BYTES;
constexpr size_t ML_BYTES = (size_t)H * NR * 2 * 4;
constexpr size_t WS_FIN = WS_ML + ML_BYTES;
constexpr size_t FIN_BYTES = (size_t)NWG * NWG * 2 * 4;
constexpr size_t WS_END = WS_FIN + FIN_BYTES;
constexpr size_t ZERO_BYTES = WS_END - WS_PART;
static_assert(ZERO_BYTES % (NWG * 16) == 0, "zero split");

constexpr unsigned SPIN_CAP = 1u << 20;

__device__ __forceinline__ float asf(unsigned u) { return __builtin_bit_cast(float, u); }
__device__ __forceinline__ float bf_lo(unsigned u) { return __builtin_bit_cast(float, u << 16); }
__device__ __forceinline__ float bf_hi(unsigned u) { return __builtin_bit_cast(float, u & 0xffff0000u); }
__device__ __forceinline__ float bf2f(u16 v) { return __builtin_bit_cast(float, (unsigned)v << 16); }
__device__ __forceinline__ u16 f2bf(float f) {
  __bf16 t = (__bf16)f;
  return __builtin_bit_cast(u16, t);
}
__device__ __forceinline__ unsigned tagf(float f) { return __builtin_bit_cast(unsigned, f) | 1u; }

__device__ __forceinline__ void unpack8(const v4u& v, float* o) {
  o[0] = bf_lo(v.x); o[1] = bf_hi(v.x); o[2] = bf_lo(v.y); o[3] = bf_hi(v.y);
  o[4] = bf_lo(v.z); o[5] = bf_hi(v.z); o[6] = bf_lo(v.w); o[7] = bf_hi(v.w);
}

__device__ __forceinline__ float wave_sum(float v) {
#pragma unroll
  for (int o = 32; o > 0; o >>= 1) v += __shfl_xor(v, o);
  return v;
}
__device__ __forceinline__ float wave_max(float v) {
#pragma unroll
  for (int o = 32; o > 0; o >>= 1) v = fmaxf(v, __shfl_xor(v, o));
  return v;
}

__global__ __launch_bounds__(NT, 2) void k1_q(const u16* __restrict__ x, const u16* __restrict__ w_q,
                                              const u16* __restrict__ w_uk, unsigned char* __restrict__ ws) {
  __shared__ float xs[MD];
  __shared__ float qred[8][D];
  __shared__ float qs[D];
  const int t = threadIdx.x, lane = t & 63, w = t >> 6;
  const int b = blockIdx.x;
  const int xcd = b & 7, idx = b >> 3;
  const int h = xcd * 4 + (idx & 3), s = idx >> 2;

  v4u wq[16];
  {
    const int l4 = lane >> 4, ch = lane & 15;
#pragma unroll
    for (int i = 0; i < 16; ++i) {
      const int row = w * 64 + i * 4 + l4;
      wq[i] = *(const v4u*)(w_q + ((size_t)row * H + h) * D + ch * 8);
    }
  }
  v4u wk[2];
#pragma unroll
  for (int i = 0; i < 2; ++i) {
    const int c = s * 64 + (t >> 4) + 32 * i;
    wk[i] = *(const v4u*)(w_uk + ((size_t)c * H + h) * D + (t & 15) * 8);
  }
  xs[t] = bf2f(x[t]);
  {
    v4u z = {0u, 0u, 0u, 0u};
    constexpr size_t per = ZERO_BYTES / NWG;
    unsigned char* zb = ws + WS_PART + (size_t)b * per;
    for (size_t o = (size_t)t * 16; o < per; o += NT * 16) *(v4u*)(zb + o) = z;
  }
  __syncthreads();
  float acc[8];
#pragma unroll
  for (int j = 0; j < 8; ++j) acc[j] = 0.f;
  {
    const int l4 = lane >> 4;
#pragma unroll
    for (int i = 0; i < 16; ++i) {
      const float xv = xs[w * 64 + i * 4 + l4];
      float f[8];
      unpack8(wq[i], f);
#pragma unroll
      for (int j = 0; j < 8; ++j) acc[j] += xv * f[j];
    }
  }
#pragma unroll
  for (int j = 0; j < 8; ++j) {
    acc[j] += __shfl_xor(acc[j], 16);
    acc[j] += __shfl_xor(acc[j], 32);
  }
  if (lane < 16) {
#pragma unroll
    for (int j = 0; j < 8; ++j) qred[w][lane * 8 + j] = acc[j];
  }
  __syncthreads();
  if (t < D) {
    float v = 0.f;
#pragma unroll
    for (int i = 0; i < 8; ++i) v += qred[i][t];
    qs[t] = v;
  }
  __syncthreads();
  const float scale = 0.08838834764831845f * 1.4426950408889634f;
  u16* qa = (u16*)(ws + WS_QA);
  float qv[8];
#pragma unroll
  for (int j = 0; j < 8; ++j) qv[j] = qs[(t & 15) * 8 + j];
#pragma unroll
  for (int i = 0; i < 2; ++i) {
    float f[8];
    unpack8(wk[i], f);
    float v = 0.f;
#pragma unroll
    for (int j = 0; j < 8; ++j) v += qv[j] * f[j];
    v += __shfl_xor(v, 1);
    v += __shfl_xor(v, 2);
    v += __shfl_xor(v, 4);
    v += __shfl_xor(v, 8);
    if ((t & 15) == 0) qa[h * DC + s * 64 + (t >> 4) + 32 * i] = f2bf(v * scale);
  }
}

__device__ __forceinline__ unsigned lds_off(unsigned row, unsigned ch) {
  return 1024u * row + 16u * (ch ^ (((row & 3) << 2) | ((row >> 2) & 3)));
}

constexpr int PSTR = 72;

__global__ __launch_bounds__(NT, 2) void k2_attn(const u16* __restrict__ x, const u16* __restrict__ c_kv,
                                                 const u16* __restrict__ w_uv, const u16* __restrict__ w_o,
                                                 const u16* __restrict__ w_gate, const u16* __restrict__ w_up,
                                                 const u16* __restrict__ w_down, const u16* __restrict__ gate_w,
                                                 unsigned char* __restrict__ ws, u16* __restrict__ out) {
  __shared__ __attribute__((aligned(16))) unsigned char ctile[KPR * 1024];
  __shared__ __attribute__((aligned(16))) float sc[2][HPG][KPR];
  __shared__ __attribute__((aligned(16))) u16 pl[HPG * PSTR];
  __shared__ __attribute__((aligned(16))) float red[8][512];
  __shared__ float small[64];
  __shared__ float latv[64];
  __shared__ float ctxv[D];
  __shared__ float hv[8];

  const int t = threadIdx.x, lane = t & 63, w = t >> 6;
  const int b = blockIdx.x;
  const int xcd = b & 7, idx = b >> 3;
  const int r = xcd * 16 + (idx >> 1), g = idx & 1;
  const int ch_ = xcd * 4 + (idx & 3), cs = idx >> 2;

  const __amdgpu_buffer_rsrc_t rsw = __builtin_amdgcn_make_buffer_rsrc((void*)ws, 0, (int)WS_END, 0x00020000);

  const u16* qa = (const u16*)(ws + WS_QA);
  v4u qf[8];
  {
    const int kh = w >> 2;
#pragma unroll
    for (int i = 0; i < 8; ++i)
      qf[i] = *(const v4u*)(qa + (g * HPG + (lane & 15)) * DC + (kh * 8 + i) * 32 + (lane >> 4) * 8);
  }
  v4u cv[8];
#pragma unroll
  for (int i = 0; i < 8; ++i) {
    const int n = t + NT * i;
    cv[i] = *(const v4u*)(c_kv + ((size_t)r * KPR + (n >> 6)) * DC + (n & 63) * 8);
  }
  v4u wuv[2];
#pragma unroll
  for (int i = 0; i < 2; ++i) {
    const int c = cs * 64 + (t >> 4) + 32 * i;
    wuv[i] = *(const v4u*)(w_uv + ((size_t)c * H + ch_) * D + (t & 15) * 8);
  }
  v4u wo[16];
#pragma unroll
  for (int i = 0; i < 16; ++i) {
    const int d = w * 16 + i;
    wo[i] = *(const v4u*)(w_o + ((size_t)ch_ * D + d) * MD + lane * 8);
  }
  const bool has_ffn = b < FF / 8;
  v4u wg = {0u, 0u, 0u, 0u}, wu = {0u, 0u, 0u, 0u}, wd = {0u, 0u, 0u, 0u};
  if (has_ffn) {
    wg = *(const v4u*)(w_gate + (size_t)t * FF + b * 8);
    wu = *(const v4u*)(w_up + (size_t)t * FF + b * 8);
    wd = *(const v4u*)(w_down + ((size_t)b * 8 + w) * MD + lane * 8);
  }
  const float xv = bf2f(x[t]);
  const float gwv = bf2f(gate_w[t]);

#pragma unroll
  for (int i = 0; i < 8; ++i) {
    const int n = t + NT * i;
    *(v4u*)(ctile + lds_off(n >> 6, n & 63)) = cv[i];
  }
  __syncthreads();

  {
    const int kt = w & 3, kh = w >> 2;
    f32x4 acc = {0.f, 0.f, 0.f, 0.f};
#pragma unroll
    for (int i = 0; i < 8; ++i) {
      const v4u bfr = *(const v4u*)(ctile + lds_off(kt * 16 + (lane & 15), (kh * 8 + i) * 4 + (lane >> 4)));
      acc = __builtin_amdgcn_mfma_f32_16x16x32_bf16(__builtin_bit_cast(bf16x8, qf[i]), __builtin_bit_cast(bf16x8, bfr), acc, 0, 0, 0);
    }
#pragma unroll
    for (int j = 0; j < 4; ++j) sc[kh][(lane >> 4) * 4 + j][kt * 16 + (lane & 15)] = acc[j];
  }
  __syncthreads();
#pragma unroll
  for (int i = 0; i < 2; ++i) {
    const int hh = 2 * w + i;
    const float sv = sc[0][hh][lane] + sc[1][hh][lane];
    const float mx = wave_max(sv);
    const float p = __builtin_amdgcn_exp2f(sv - mx);
    const u16 pb = f2bf(p);
    const float l = wave_sum(bf2f(pb));
    pl[hh * PSTR + lane] = pb;
    if (lane == 0) {
      v2u o = {tagf(mx), tagf(l)};
      __builtin_amdgcn_raw_buffer_store_b64(o, rsw, (int)(WS_ML + ((size_t)(g * HPG + hh) * NR + r) * 8), 0, 16);
    }
  }
  __syncthreads();
  {
    v4u pf[2];
#pragma unroll
    for (int ks = 0; ks < 2; ++ks) pf[ks] = *(const v4u*)(pl + (lane & 15) * PSTR + ks * 32 + (lane >> 4) * 8);
    const unsigned cbase = (unsigned)(size_t)ctile;
    const unsigned gq = lane >> 4, q = (lane & 15) >> 2, p = lane & 3;
#pragma unroll
    for (int nt = 0; nt < 4; ++nt) {
      f32x4 acc = {0.f, 0.f, 0.f, 0.f};
      const unsigned cc = w * 4 + nt;
#pragma unroll
      for (int ks = 0; ks < 2; ++ks) {
        u16x4 a0, a1;
        const unsigned ad0 = cbase + lds_off(32 * ks + 8 * gq + q, 2 * cc + (p >> 1)) + 8 * (p & 1);
        const unsigned ad1 = cbase + lds_off(32 * ks + 8 * gq + 4 + q, 2 * cc + (p >> 1)) + 8 * (p & 1);
        asm volatile("ds_read_b64_tr_b16 %0, %2\n\tds_read_b64_tr_b16 %1, %3\n\ts_waitcnt lgkmcnt(0)"
                     : "=&v"(a0), "=&v"(a1) : "v"(ad0), "v"(ad1) : "memory");
        bf16x8 af;
        af[0] = a0[0]; af[1] = a0[1]; af[2] = a0[2]; af[3] = a0[3];
        af[4] = a1[0]; af[5] = a1[1]; af[6] = a1[2]; af[7] = a1[3];
        acc = __builtin_amdgcn_mfma_f32_16x16x32_bf16(af, __builtin_bit_cast(bf16x8, pf[ks]), acc, 0, 0, 0);
      }
      v4u o = {tagf(acc[0]), tagf(acc[1]), tagf(acc[2]), tagf(acc[3])};
      const size_t off = WS_PART + ((((size_t)(g * HPG + (lane & 15)) * 8 + w) * NR + r) * 64 + nt * 16 + (lane >> 4) * 4) * 4;
      __builtin_amdgcn_raw_buffer_store_b128(o, rsw, (int)off, 0, 16);
    }
  }

  float gatev;
  {
    float gp = wave_sum(xv * gwv);
    if (lane == 0) small[w] = gp;
    float fg[8], fu[8];
    unpack8(wg, fg);
    unpack8(wu, fu);
#pragma unroll
    for (int j = 0; j < 8; ++j) {
      fg[j] = wave_sum(fg[j] * xv);
      fu[j] = wave_sum(fu[j] * xv);
    }
    if (lane == 0) {
#pragma unroll
      for (int j = 0; j < 8; ++j) { red[w][j] = fg[j]; red[w][8 + j] = fu[j]; }
    }
    __syncthreads();
    float gs = 0.f;
#pragma unroll
    for (int i = 0; i < 8; ++i) gs += small[i];
    gatev = 1.f / (1.f + __expf(-gs));
    if (t < 8) {
      float a = 0.f, u = 0.f;
#pragma unroll
      for (int i = 0; i < 8; ++i) { a += red[i][t]; u += red[i][8 + t]; }
      hv[t] = a / (1.f + __expf(-a)) * u;
    }
    __syncthreads();
  }

  {
    float mm[2], ll[2];
    v4u pv[4];
    unsigned spins = 0;
    int zoff = 0;
    for (;;) {
      bool ok = true;
      asm volatile("" : "+v"(zoff));
#pragma unroll
      for (int i = 0; i < 2; ++i) {
        const v2u v = __builtin_amdgcn_raw_buffer_load_b64(rsw, zoff + (int)(WS_ML + ((size_t)ch_ * NR + i * 64 + lane) * 8), 0, 16);
        ok &= (v.x & v.y & 1u) != 0;
        mm[i] = asf(v.x);
        ll[i] = asf(v.y);
      }
#pragma unroll
      for (int i = 0; i < 4; ++i) {
        const int rr = w * 16 + i * 4 + (lane >> 4);
        pv[i] = __builtin_amdgcn_raw_buffer_load_b128(rsw, zoff + (int)(WS_PART + ((((size_t)ch_ * 8 + cs) * NR + rr) * 64 + (lane & 15) * 4) * 4), 0, 16);
        ok &= (pv[i].x & pv[i].y & pv[i].z & pv[i].w & 1u) != 0;
      }
      if (__all(ok)) break;
      if (++spins > SPIN_CAP) break;
      __builtin_amdgcn_s_sleep(1);
      asm volatile("" ::: "memory");
    }
    const float M = wave_max(fmaxf(mm[0], mm[1]));
    const float e0 = __builtin_amdgcn_exp2f(mm[0] - M), e1 = __builtin_amdgcn_exp2f(mm[1] - M);
    const float L = wave_sum(e0 * ll[0] + e1 * ll[1]);
    const float inv = 1.f / L;
    float a4[4] = {0.f, 0.f, 0.f, 0.f};
#pragma unroll
    for (int i = 0; i < 4; ++i) {
      const int rr = w * 16 + i * 4 + (lane >> 4);
      const float eA = __shfl(e0, rr & 63), eB = __shfl(e1, rr & 63);
      const float sc_ = (rr < 64 ? eA : eB) * inv;
      a4[0] += sc_ * asf(pv[i].x);
      a4[1] += sc_ * asf(pv[i].y);
      a4[2] += sc_ * asf(pv[i].z);
      a4[3] += sc_ * asf(pv[i].w);
    }
#pragma unroll
    for (int j = 0; j < 4; ++j) {
      a4[j] += __shfl_xor(a4[j], 16);
      a4[j] += __shfl_xor(a4[j], 32);
    }
    if (lane < 16) {
#pragma unroll
      for (int j = 0; j < 4; ++j) red[w][lane * 4 + j] = a4[j];
    }
    __syncthreads();
    if (t < 64) {
      float v = 0.f;
#pragma unroll
      for (int i = 0; i < 8; ++i) v += red[i][t];
      latv[t] = v;
    }
    __syncthreads();
  }
  {
    float a8[8];
#pragma unroll
    for (int j = 0; j < 8; ++j) a8[j] = 0.f;
#pragma unroll
    for (int i = 0; i < 2; ++i) {
      const float lv = latv[(t >> 4) + 32 * i];
      float f[8];
      unpack8(wuv[i], f);
#pragma unroll
      for (int j = 0; j < 8; ++j) a8[j] += lv * f[j];
    }
#pragma unroll
    for (int j = 0; j < 8; ++j) {
      a8[j] += __shfl_xor(a8[j], 16);
      a8[j] += __shfl_xor(a8[j], 32);
    }
    if (lane < 16) {
#pragma unroll
      for (int j = 0; j < 8; ++j) red[w][lane * 8 + j] = a8[j];
    }
    __syncthreads();
    if (t < D) {
      float v = 0.f;
#pragma unroll
      for (int i = 0; i < 8; ++i) v += red[i][t];
      ctxv[t] = v;
    }
    __syncthreads();
  }
  {
    float a8[8];
    const float hw = has_ffn ? hv[w] * (1.f - gatev) : 0.f;
    {
      float f[8];
      unpack8(wd, f);
#pragma unroll
      for (int j = 0; j < 8; ++j) a8[j] = hw * f[j];
    }
#pragma unroll
    for (int i = 0; i < 16; ++i) {
      const float cvv = ctxv[w * 16 + i] * gatev;
      float f[8];
      unpack8(wo[i], f);
#pragma unroll
      for (int j = 0; j < 8; ++j) a8[j] += cvv * f[j];
    }
#pragma unroll
    for (int j = 0; j < 8; ++j) red[w][lane * 8 + j] = a8[j];
    __syncthreads();
    if (t < 256) {
      float v0 = 0.f, v1 = 0.f;
#pragma unroll
      for (int i = 0; i < 8; ++i) { v0 += red[i][2 * t]; v1 += red[i][2 * t + 1]; }
      v2u o = {tagf(v0), tagf(v1)};
      __builtin_amdgcn_raw_buffer_store_b64(o, rsw, (int)(WS_FIN + ((size_t)t * NWG + b) * 8), 0, 16);
    }
  }
  {
    float v0 = 0.f, v1 = 0.f;
    if (t < 256) {
      unsigned spins = 0;
      int zoff = 0;
      for (;;) {
        asm volatile("" : "+v"(zoff));
        const v2u v = __builtin_amdgcn_raw_buffer_load_b64(rsw, zoff + (int)(WS_FIN + ((size_t)b * NWG + t) * 8), 0, 16);
        const bool ok = (v.x & v.y & 1u) != 0;
        v0 = asf(v.x);
        v1 = asf(v.y);
        if (__all(ok)) break;
        if (++spins > SPIN_CAP) break;
        __builtin_amdgcn_s_sleep(1);
        asm volatile("" ::: "memory");
      }
      v0 = wave_sum(v0);
      v1 = wave_sum(v1);
      if (lane == 0) { small[32 + 2 * w] = v0; small[33 + 2 * w] = v1; }
    }
    __syncthreads();
    if (t == 0) {
      const float o0 = small[32] + small[34] + small[36] + small[38];
      const float o1 = small[33] + small[35] + small[37] + small[39];
      *(unsigned*)(out + 2 * b) = (unsigned)f2bf(o0) | ((unsigned)f2bf(o1) << 16);
    }
  }
}

extern "C" void kernel_launch(void* const* d_in, const int* in_sizes, int n_in, void* d_out, int out_size,
                              void* d_ws, size_t ws_size, hipStream_t stream) {
  const u16* x = (const u16*)d_in[0];
  const u16* c_kv = (const u16*)d_in[1];
  const u16* w_q = (const u16*)d_in[2];
  const u16* w_uk = (const u16*)d_in[3];
  const u16* w_uv = (const u16*)d_in[4];
  const u16* w_o = (const u16*)d_in[5];
  const u16* w_gate = (const u16*)d_in[7];
  const u16* w_up = (const u16*)d_in[8];
  const u16* w_down = (const u16*)d_in[9];
  const u16* gate_w = (const u16*)d_in[10];
  unsigned char* ws = (unsigned char*)d_ws;
  k1_q<<<NWG, NT, 0, stream>>>(x, w_q, w_uk, ws);
  k2_attn<<<NWG, NT, 0, stream>>>(x, c_kv, w_uv, w_o, w_gate, w_up, w_down, gate_w, ws, (u16*)d_out);
}
```

```cpp
#include <hip/hip_runtime.h>
#include <hip/hip_bf16.h>

typedef unsigned short u16;
typedef unsigned v4u __attribute__((ext_vector_type(4)));
typedef unsigned v2u __attribute__((ext_vector_type(2)));
typedef float f32x4 __attribute__((ext_vector_type(4)));
typedef short bf16x8 __attribute__((ext_vector_type(8)));
typedef unsigned short u16x4 __attribute__((ext_vector_type(4)));

constexpr int H = 32, D = 128, MD = 512, DC = 512, SK = 8192, FF = 1376;
constexpr int NWG = 256, NT = 512;
constexpr int HG = 2;
constexpr int HPG = H / HG;
constexpr int NR = NWG / HG;
constexpr int KPR = SK / NR;
static_assert(HPG == 16 && KPR == 64, "geometry");

constexpr size_t WS_QA = 0;
constexpr int QREP = 8;
constexpr size_t QA_ONE = (size_t)H * DC * 2;
constexpr size_t QA_BYTES = QA_ONE * QREP;
constexpr size_t WS_FLG = WS_QA + QA_BYTES;
constexpr size_t FLG_BYTES = (size_t)H * 8 * NR * 4;
constexpr size_t WS_FIN = WS_FLG + FLG_BYTES;
constexpr size_t FIN_BYTES = (size_t)NWG * NWG * 2 * 4;
constexpr size_t ZERO_BYTES = WS_FIN + FIN_BYTES;
constexpr size_t WS_QP = ZERO_BYTES;
constexpr size_t QP_BYTES = (size_t)H * 8 * D * 4;
constexpr size_t WS_HV = WS_QP + QP_BYTES;
constexpr size_t HV_BYTES = 8192;
constexpr size_t WS_PART = WS_HV + HV_BYTES;
constexpr size_t PART_BYTES = (size_t)H * 8 * NR * 64 * 4;
constexpr size_t WS_ML = WS_PART + PART_BYTES;
constexpr size_t ML_BYTES = (size_t)H * 8 * NR * 2 * 4;
constexpr size_t WS_END = WS_ML + ML_BYTES;
static_assert(ZERO_BYTES % (NWG * 16) == 0, "zero split");

constexpr unsigned SPIN_CAP = 1u << 20;
#define STAMP(k, i) do { } while (0)

__device__ __forceinline__ float asf(unsigned u) { return __builtin_bit_cast(float, u); }
__device__ __forceinline__ unsigned asu(float f) { return __builtin_bit_cast(unsigned, f); }
__device__ __forceinline__ float bf_lo(unsigned u) { return __builtin_bit_cast(float, u << 16); }
__device__ __forceinline__ float bf_hi(unsigned u) { return __builtin_bit_cast(float, u & 0xffff0000u); }
__device__ __forceinline__ float bf2f(u16 v) { return __builtin_bit_cast(float, (unsigned)v << 16); }
__device__ __forceinline__ u16 f2bf(float f) {
  __bf16 t = (__bf16)f;
  return __builtin_bit_cast(u16, t);
}
__device__ __forceinline__ unsigned pack2(float a, float b) { return (unsigned)f2bf(a) | ((unsigned)f2bf(b) << 16); }
__device__ __forceinline__ unsigned tagf(float f) { return __builtin_bit_cast(unsigned, f) | 1u; }

__device__ __forceinline__ void unpack8(const v4u& v, float* o) {
  o[0] = bf_lo(v.x); o[1] = bf_hi(v.x); o[2] = bf_lo(v.y); o[3] = bf_hi(v.y);
  o[4] = bf_lo(v.z); o[5] = bf_hi(v.z); o[6] = bf_lo(v.w); o[7] = bf_hi(v.w);
}

template <int CTRL> __device__ __forceinline__ float dppf(float v) {
  return __builtin_bit_cast(float, __builtin_amdgcn_update_dpp(0, __builtin_bit_cast(int, v), CTRL, 0xf, 0xf, false));
}
__device__ __forceinline__ float row_sum(float v) {
  v += dppf<0x128>(v); v += dppf<0x124>(v); v += dppf<0x122>(v); v += dppf<0x121>(v);
  return v;
}
__device__ __forceinline__ float row_max(float v) {
  v = fmaxf(v, dppf<0x128>(v)); v = fmaxf(v, dppf<0x124>(v)); v = fmaxf(v, dppf<0x122>(v)); v = fmaxf(v, dppf<0x121>(v));
  return v;
}
__device__ __forceinline__ float rl(float v, int l) {
  return __builtin_bit_cast(float, __builtin_amdgcn_readlane(__builtin_bit_cast(int, v), l));
}
__device__ __forceinline__ float wave_sum(float v) {
  v = row_sum(v);
  return (rl(v, 0) + rl(v, 16)) + (rl(v, 32) + rl(v, 48));
}
__device__ __forceinline__ float wave_max(float v) {
  v = row_max(v);
  return fmaxf(fmaxf(rl(v, 0), rl(v, 16)), fmaxf(rl(v, 32), rl(v, 48)));
}
__device__ __forceinline__ float xrow_sum(float v) {
  auto a = __builtin_amdgcn_permlane16_swap(asu(v), asu(v), false, false);
  v = asf(a[0]) + asf(a[1]);
  auto b = __builtin_amdgcn_permlane32_swap(asu(v), asu(v), false, false);
  return asf(b[0]) + asf(b[1]);
}

__global__ __launch_bounds__(NT, 2) void k1_q(const u16* __restrict__ x, const u16* __restrict__ w_q,
                                              const u16* __restrict__ w_gate, const u16* __restrict__ w_up,
                                              unsigned char* __restrict__ ws) {
  __shared__ __attribute__((aligned(16))) float qred[8][D];
  __shared__ __attribute__((aligned(16))) float fred[8][16];
  const int t = threadIdx.x, lane = t & 63, w = t >> 6;
  const int b = blockIdx.x;
  const int h = b >> 3, s = b & 7;
  STAMP(0, 0);

  v4u wq[2];
  float xq[2];
#pragma unroll
  for (int i = 0; i < 2; ++i) {
    const int m = s * 64 + (t >> 4) + 32 * i;
    wq[i] = *(const v4u*)(w_q + ((size_t)m * H + h) * D + (t & 15) * 8);
    xq[i] = bf2f(x[m]);
  }
  const bool has_ffn = b < FF / 8;
  v4u wg = {0u, 0u, 0u, 0u}, wu = {0u, 0u, 0u, 0u};
  if (has_ffn) {
    wg = *(const v4u*)(w_gate + (size_t)t * FF + b * 8);
    wu = *(const v4u*)(w_up + (size_t)t * FF + b * 8);
  }
  const float xv = bf2f(x[t]);
  {
    v4u z = {0u, 0u, 0u, 0u};
    constexpr size_t per = ZERO_BYTES / NWG;
    unsigned char* zb = ws + (size_t)b * per;
    for (size_t o = (size_t)t * 16; o < per; o += NT * 16) *(v4u*)(zb + o) = z;
  }
  STAMP(0, 1);
  {
    float f0[8], f1[8];
    unpack8(wq[0], f0);
    unpack8(wq[1], f1);
    f32x4 o0, o1;
#pragma unroll
    for (int j = 0; j < 4; ++j) {
      o0[j] = xrow_sum(xq[0] * f0[j] + xq[1] * f1[j]);
      o1[j] = xrow_sum(xq[0] * f0[4 + j] + xq[1] * f1[4 + j]);
    }
    if (lane < 16) {
      *(f32x4*)&qred[w][lane * 8] = o0;
      *(f32x4*)&qred[w][lane * 8 + 4] = o1;
    }
  }
  if (has_ffn) {
    float fg[8], fu[8];
    unpack8(wg, fg);
    unpack8(wu, fu);
#pragma unroll
    for (int j = 0; j < 8; ++j) { fg[j] = xrow_sum(row_sum(fg[j] * xv)); fu[j] = xrow_sum(row_sum(fu[j] * xv)); }
    if (lane == 0) {
#pragma unroll
      for (int j = 0; j < 8; ++j) { fred[w][j] = fg[j]; fred[w][8 + j] = fu[j]; }
    }
  }
  __syncthreads();
  STAMP(0, 2);
  if (t < D) {
    float v = 0.f;
#pragma unroll
    for (int i = 0; i < 8; ++i) v += qred[i][t];
    ((float*)(ws + WS_QP))[(h * 8 + s) * D + t] = v;
  } else if (has_ffn && t >= 192 && t < 200) {
    const int j = t - 192;
    float a = 0.f, u = 0.f;
#pragma unroll
    for (int i = 0; i < 8; ++i) { a += fred[i][j]; u += fred[i][8 + j]; }
    ((float*)(ws + WS_HV))[b * 8 + j] = a / (1.f + __expf(-a)) * u;
  }
  STAMP(0, 3);
}

__device__ __forceinline__ unsigned lds_off(unsigned row, unsigned ch) {
  return 1024u * row + 16u * (ch ^ (((row & 3) << 2) | ((row >> 2) & 3)));
}

constexpr int PSTR = 72;
constexpr int QSTR = 520;

__global__ __launch_bounds__(NT, 2) void k2_attn(const u16* __restrict__ x, const u16* __restrict__ c_kv,
                                                 const u16* __restrict__ w_uk, const u16* __restrict__ w_uv,
                                                 const u16* __restrict__ w_o, const u16* __restrict__ w_down,
                                                 const u16* __restrict__ gate_w, unsigned char* __restrict__ ws,
                                                 u16* __restrict__ out) {
  __shared__ __attribute__((aligned(16))) unsigned char ctile[KPR * 1024];
  __shared__ __attribute__((aligned(16))) u16 qb[HPG * QSTR];
  __shared__ __attribute__((aligned(16))) float sc[2][HPG][KPR];
  __shared__ __attribute__((aligned(16))) u16 pl[HPG * PSTR];
  __shared__ __attribute__((aligned(16))) float red[8 * 512];
  __shared__ __attribute__((aligned(16))) float red2[8 * 128];
  __shared__ __attribute__((aligned(16))) float red1[8 * 64];
  __shared__ __attribute__((aligned(16))) float qs[D];
  __shared__ __attribute__((aligned(16))) float qav[64];
  __shared__ __attribute__((aligned(16))) float mlv[HPG * 2];
  __shared__ float small[64];

  const int t = threadIdx.x, lane = t & 63, w = t >> 6;
  const int b = blockIdx.x;
  const int xcd = b & 7, idx = b >> 3;
  const int r = xcd * 16 + (idx >> 1), g = idx & 1;
  const int ch_ = xcd * 4 + (idx & 3), cs = idx >> 2;

  const __amdgpu_buffer_rsrc_t rsw = __builtin_amdgcn_make_buffer_rsrc((void*)ws, 0, (int)WS_END, 0x00020000);

  STAMP(1, 0);
  float qp[8];
  if (t < D) {
#pragma unroll
    for (int i = 0; i < 8; ++i) qp[i] = ((const float*)(ws + WS_QP))[(ch_ * 8 + i) * D + t];
  }
  v4u wk[2];
#pragma unroll
  for (int i = 0; i < 2; ++i) {
    const int c = cs * 64 + (t >> 4) + 32 * i;
    wk[i] = *(const v4u*)(w_uk + ((size_t)c * H + ch_) * D + (t & 15) * 8);
  }
  __builtin_amdgcn_s_barrier();
  v4u cv[8];
#pragma unroll
  for (int i = 0; i < 8; ++i) {
    const int n = t + NT * i;
    cv[i] = *(const v4u*)(c_kv + ((size_t)r * KPR + (n >> 6)) * DC + (n & 63) * 8);
  }
  if (t < D) {
    qs[t] = ((qp[0] + qp[1]) + (qp[2] + qp[3])) + ((qp[4] + qp[5]) + (qp[6] + qp[7]));
  }
  __syncthreads();
  {
    const float scale = 0.08838834764831845f * 1.4426950408889634f;
    const f32x4 q0 = *(const f32x4*)&qs[(t & 15) * 8], q1 = *(const f32x4*)&qs[(t & 15) * 8 + 4];
#pragma unroll
    for (int i = 0; i < 2; ++i) {
      float f[8];
      unpack8(wk[i], f);
      float v = (q0[0] * f[0] + q0[1] * f[1]) + (q0[2] * f[2] + q0[3] * f[3]) + (q1[0] * f[4] + q1[1] * f[5]) + (q1[2] * f[6] + q1[3] * f[7]);
      v = row_sum(v);
      if ((t & 15) == 0) qav[(t >> 4) + 32 * i] = v * scale;
    }
    __syncthreads();
    if (t < 32 * QREP) {
      const int tt = t & 31, rep = t >> 5;
      const unsigned o = pack2(qav[2 * tt], qav[2 * tt + 1]) | 1u;
      __builtin_amdgcn_raw_buffer_store_b32(o, rsw, (int)(WS_QA + rep * QA_ONE + (ch_ * DC + cs * 64 + 2 * tt) * 2), 0, 16);
    }
  }
  STAMP(1, 1);
#pragma unroll
  for (int i = 0; i < 8; ++i) {
    const int n = t + NT * i;
    *(v4u*)(ctile + lds_off(n >> 6, n & 63)) = cv[i];
  }
  STAMP(1, 2);
  {
    v4u qv[2];
    unsigned spins = 0;
    int zoff = 0;
    for (;;) {
      bool ok = true;
      asm volatile("" : "+v"(zoff));
#pragma unroll
      for (int i = 0; i < 2; ++i) {
        qv[i] = __builtin_amdgcn_raw_buffer_load_b128(rsw, zoff + (int)(WS_QA + xcd * QA_ONE + (g * HPG * DC + (i * NT + t) * 8) * 2), 0, 16);
        ok &= (qv[i].x & qv[i].y & qv[i].z & qv[i].w & 1u) != 0;
      }
      if (__all(ok)) break;
      if (++spins > SPIN_CAP) break;
      __builtin_amdgcn_s_sleep(SLP);
    }
#pragma unroll
    for (int i = 0; i < 2; ++i) {
      const int e = (i * NT + t) * 8;
      *(v4u*)(qb + (e >> 9) * QSTR + (e & 511)) = qv[i];
    }
  }
  v4u wuv[2];
#pragma unroll
  for (int i = 0; i < 2; ++i) {
    const int c = cs * 64 + (t >> 4) + 32 * i;
    wuv[i] = *(const v4u*)(w_uv + ((size_t)c * H + ch_) * D + (t & 15) * 8);
  }
  const bool has_ffn = b < FF / 8;
  const int fb = has_ffn ? b : 0;
  const v4u wd = *(const v4u*)(w_down + ((size_t)fb * 8 + w) * MD + lane * 8);
  const float hw_raw = ((const float*)(ws + WS_HV))[fb * 8 + w];
  const u16 x_raw = x[t], gw_raw = gate_w[t];
  v4u wo[16];
#pragma unroll
  for (int i = 0; i < 16; ++i) {
    const int d = w * 16 + i;
    wo[i] = *(const v4u*)(w_o + ((size_t)ch_ * D + d) * MD + lane * 8);
  }
  __syncthreads();
  STAMP(1, 3);

  {
    const int kt = w & 3, kh = w >> 2;
    f32x4 acc = {0.f, 0.f, 0.f, 0.f};
#pragma unroll
    for (int i = 0; i < 8; ++i) {
      const v4u afr = *(const v4u*)(qb + (lane & 15) * QSTR + (kh * 8 + i) * 32 + (lane >> 4) * 8);
      const v4u bfr = *(const v4u*)(ctile + lds_off(kt * 16 + (lane & 15), (kh * 8 + i) * 4 + (lane >> 4)));
      acc = __builtin_amdgcn_mfma_f32_16x16x32_bf16(__builtin_bit_cast(bf16x8, afr), __builtin_bit_cast(bf16x8, bfr), acc, 0, 0, 0);
    }
#pragma unroll
    for (int j = 0; j < 4; ++j) sc[kh][(lane >> 4) * 4 + j][kt * 16 + (lane & 15)] = acc[j];
  }
  __syncthreads();
#pragma unroll
  for (int i = 0; i < 2; ++i) {
    const int hh = 2 * w + i;
    const float sv = sc[0][hh][lane] + sc[1][hh][lane];
    const float mx = wave_max(sv);
    const float p = __builtin_amdgcn_exp2f(sv - mx);
    const u16 pb = f2bf(p);
    const float l = wave_sum(bf2f(pb));
    pl[hh * PSTR + lane] = pb;
    if (lane == 0) { mlv[2 * hh] = mx; mlv[2 * hh + 1] = l; }
  }
  __syncthreads();
  {
    v4u pf[2];
#pragma unroll
    for (int ks = 0; ks < 2; ++ks) pf[ks] = *(const v4u*)(pl + (lane & 15) * PSTR + ks * 32 + (lane >> 4) * 8);
    const unsigned cbase = (unsigned)(size_t)ctile;
    const unsigned gq = lane >> 4, q = (lane & 15) >> 2, p = lane & 3;
    u16x4 a[4][4];
#pragma unroll
    for (int nt = 0; nt < 4; ++nt) {
      const unsigned cc = w * 4 + nt;
      const unsigned ad0 = cbase + lds_off(8 * gq + q, 2 * cc + (p >> 1)) + 8 * (p & 1);
      const unsigned ad1 = cbase + lds_off(8 * gq + 4 + q, 2 * cc + (p >> 1)) + 8 * (p & 1);
      asm volatile("ds_read_b64_tr_b16 %0, %4\n\tds_read_b64_tr_b16 %1, %5\n\tds_read_b64_tr_b16 %2, %4 offset:32768\n\tds_read_b64_tr_b16 %3, %5 offset:32768"
                   : "=&v"(a[nt][0]), "=&v"(a[nt][1]), "=&v"(a[nt][2]), "=&v"(a[nt][3]) : "v"(ad0), "v"(ad1) : "memory");
    }
    if (lane < 16) {
      const v2u o = {asu(mlv[2 * lane]), asu(mlv[2 * lane + 1])};
      __builtin_amdgcn_raw_buffer_store_b64(o, rsw, (int)(WS_ML + (((size_t)(g * HPG + lane) * 8 + w) * NR + r) * 8), 0, 16);
    }
    asm volatile("s_waitcnt lgkmcnt(0)" ::: "memory");
#pragma unroll
    for (int nt = 0; nt < 4; ++nt) {
      asm volatile("" : "+v"(a[nt][0]), "+v"(a[nt][1]), "+v"(a[nt][2]), "+v"(a[nt][3]));
      f32x4 acc = {0.f, 0.f, 0.f, 0.f};
#pragma unroll
      for (int ks = 0; ks < 2; ++ks) {
        bf16x8 af;
        af[0] = a[nt][2 * ks][0]; af[1] = a[nt][2 * ks][1]; af[2] = a[nt][2 * ks][2]; af[3] = a[nt][2 * ks][3];
        af[4] = a[nt][2 * ks + 1][0]; af[5] = a[nt][2 * ks + 1][1]; af[6] = a[nt][2 * ks + 1][2]; af[7] = a[nt][2 * ks + 1][3];
        acc = __builtin_amdgcn_mfma_f32_16x16x32_bf16(af, __builtin_bit_cast(bf16x8, pf[ks]), acc, 0, 0, 0);
      }
      const size_t off = WS_PART + ((((size_t)(g * HPG + (lane & 15)) * 8 + w) * NR + r) * 64 + nt * 16 + (lane >> 4) * 4) * 4;
      __builtin_amdgcn_raw_buffer_store_b128(__builtin_bit_cast(v4u, acc), rsw, (int)off, 0, 16);
    }
    asm volatile("s_waitcnt vmcnt(0)" ::: "memory");
    if (lane < 16) __builtin_amdgcn_raw_buffer_store_b32(1u, rsw, (int)(WS_FLG + (((g * HPG + lane) * 8 + w) * NR + r) * 4), 0, 16);
  }
  STAMP(1, 4);
  {
    const float gp = wave_sum(bf2f(x_raw) * bf2f(gw_raw));
    if (lane == 0) small[w] = gp;
  }

  {
    int zoff = 0;
    if (w == 0) {
      unsigned spins = 0;
      for (;;) {
        asm volatile("" : "+v"(zoff));
        const v2u f = __builtin_amdgcn_raw_buffer_load_b64(rsw, zoff + (int)(WS_FLG + ((ch_ * 8 + cs) * NR + lane * 2) * 4), 0, 16);
        if (__all((f.x & f.y) != 0)) break;
        if (++spins > SPIN_CAP) break;
        __builtin_amdgcn_s_sleep(SLP);
      }
    }
    __syncthreads();
    asm volatile("" : "+v"(zoff));
    STAMP(1, 5);
    float mm[2], ll[2];
    v4u pv[4];
#pragma unroll
    for (int i = 0; i < 2; ++i) {
      const v2u v = __builtin_amdgcn_raw_buffer_load_b64(rsw, zoff + (int)(WS_ML + (((size_t)ch_ * 8 + cs) * NR + i * 64 + lane) * 8), 0, 16);
      mm[i] = asf(v.x);
      ll[i] = asf(v.y);
    }
#pragma unroll
    for (int i = 0; i < 4; ++i) {
      const int rr = w * 16 + i * 4 + (lane >> 4);
      pv[i] = __builtin_amdgcn_raw_buffer_load_b128(rsw, zoff + (int)(WS_PART + ((((size_t)ch_ * 8 + cs) * NR + rr) * 64 + (lane & 15) * 4) * 4), 0, 16);
    }
    const float M = wave_max(fmaxf(mm[0], mm[1]));
    const float e0 = __builtin_amdgcn_exp2f(mm[0] - M), e1 = __builtin_amdgcn_exp2f(mm[1] - M);
    const float L = wave_sum(e0 * ll[0] + e1 * ll[1]);
    const float inv = 1.f / L;
    const float esel = ((w < 4) ? e0 : e1) * inv;
    f32x4 a4 = {0.f, 0.f, 0.f, 0.f};
#pragma unroll
    for (int i = 0; i < 4; ++i) {
      const float sc_ = __shfl(esel, (w & 3) * 16 + i * 4 + (lane >> 4));
      a4[0] += sc_ * asf(pv[i].x);
      a4[1] += sc_ * asf(pv[i].y);
      a4[2] += sc_ * asf(pv[i].z);
      a4[3] += sc_ * asf(pv[i].w);
    }
#pragma unroll
    for (int j = 0; j < 4; ++j) a4[j] = xrow_sum(a4[j]);
    if (lane < 16) *(f32x4*)&red1[w * 64 + lane * 4] = a4;
    __syncthreads();
  }
  STAMP(1, 6);
  {
    float l0 = 0.f, l1 = 0.f;
#pragma unroll
    for (int i = 0; i < 8; ++i) { l0 += red1[i * 64 + (t >> 4)]; l1 += red1[i * 64 + (t >> 4) + 32]; }
    float f0[8], f1[8];
    unpack8(wuv[0], f0);
    unpack8(wuv[1], f1);
    f32x4 o0, o1;
#pragma unroll
    for (int j = 0; j < 4; ++j) {
      o0[j] = xrow_sum(l0 * f0[j] + l1 * f1[j]);
      o1[j] = xrow_sum(l0 * f0[4 + j] + l1 * f1[4 + j]);
    }
    if (lane < 16) {
      *(f32x4*)&red2[w * D + lane * 8] = o0;
      *(f32x4*)&red2[w * D + lane * 8 + 4] = o1;
    }
    __syncthreads();
  }
  {
    float gs = 0.f;
#pragma unroll
    for (int i = 0; i < 8; ++i) gs += small[i];
    const float gatev = 1.f / (1.f + __expf(-gs));
    f32x4 cx[4];
#pragma unroll
    for (int q4 = 0; q4 < 4; ++q4) {
      f32x4 v = *(const f32x4*)&red2[w * 16 + q4 * 4];
#pragma unroll
      for (int i = 1; i < 8; ++i) v += *(const f32x4*)&red2[i * D + w * 16 + q4 * 4];
      cx[q4] = v * gatev;
    }
    float a8[8];
    {
      const float hh = has_ffn ? hw_raw * (1.f - gatev) : 0.f;
      float f[8];
      unpack8(wd, f);
#pragma unroll
      for (int j = 0; j < 8; ++j) a8[j] = hh * f[j];
    }
#pragma unroll
    for (int i = 0; i < 16; ++i) {
      const float cvv = cx[i >> 2][i & 3];
      float f[8];
      unpack8(wo[i], f);
#pragma unroll
      for (int j = 0; j < 8; ++j) a8[j] += cvv * f[j];
    }
    *(f32x4*)&red[w * 512 + lane * 8] = f32x4{a8[0], a8[1], a8[2], a8[3]};
    *(f32x4*)&red[w * 512 + lane * 8 + 4] = f32x4{a8[4], a8[5], a8[6], a8[7]};
    __syncthreads();
    if (t < 256) {
      float v0 = 0.f, v1 = 0.f;
#pragma unroll
      for (int i = 0; i < 8; ++i) { v0 += red[i * 512 + 2 * t]; v1 += red[i * 512 + 2 * t + 1]; }
      v2u o = {tagf(v0), tagf(v1)};
      __builtin_amdgcn_raw_buffer_store_b64(o, rsw, (int)(WS_FIN + ((size_t)t * NWG + b) * 8), 0, 16);
    }
  }
  STAMP(1, 7);
  {
    float v0 = 0.f, v1 = 0.f;
    if (t < 256) {
      unsigned spins = 0;
      int zoff = 0;
      for (;;) {
        asm volatile("" : "+v"(zoff));
        const v2u v = __builtin_amdgcn_raw_buffer_load_b64(rsw, zoff + (int)(WS_FIN + ((size_t)b * NWG + t) * 8), 0, 16);
        const bool ok = (v.x & v.y & 1u) != 0;
        v0 = asf(v.x);
        v1 = asf(v.y);
        if (__all(ok)) break;
        if (++spins > SPIN_CAP) break;
        __builtin_amdgcn_s_sleep(SLP);
      }
      STAMP(1, 8);
      v0 = wave_sum(v0);
      v1 = wave_sum(v1);
      if (lane == 0) { small[32 + 2 * w] = v0; small[33 + 2 * w] = v1; }
    }
    __syncthreads();
    if (t == 0) {
      const float o0 = (small[32] + small[34]) + (small[36] + small[38]);
      const float o1 = (small[33] + small[35]) + (small[37] + small[39]);
      *(unsigned*)(out + 2 * b) = pack2(o0, o1);
    }
  }
}

extern "C" void kernel_launch(void* const* d_in, const int* in_sizes, int n_in, void* d_out, int out_size,
                              void* d_ws, size_t ws_size, hipStream_t stream) {
  const u16* x = (const u16*)d_in[0];
  const u16* c_kv = (const u16*)d_in[1];
  const u16* w_q = (const u16*)d_in[2];
  const u16* w_uk = (const u16*)d_in[3];
  const u16* w_uv = (const u16*)d_in[4];
  const u16* w_o = (const u16*)d_in[5];
  const u16* w_gate = (const u16*)d_in[7];
  const u16* w_up = (const u16*)d_in[8];
  const u16* w_down = (const u16*)d_in[9];
  const u16* gate_w = (const u16*)d_in[10];
  unsigned char* ws = (unsigned char*)d_ws;
  k1_q<<<NWG, NT, 0, stream>>>(x, w_q, w_gate, w_up, ws);
  k2_attn<<<NWG, NT, 0, stream>>>(x, c_kv, w_uk, w_uv, w_o, w_down, gate_w, ws, (u16*)d_out);
}
```

```cpp
#include <hip/hip_runtime.h>
#include <hip/hip_bf16.h>

typedef unsigned short u16;
typedef unsigned v4u __attribute__((ext_vector_type(4)));
typedef unsigned v2u __attribute__((ext_vector_type(2)));
typedef float f32x4 __attribute__((ext_vector_type(4)));
typedef short bf16x8 __attribute__((ext_vector_type(8)));
typedef unsigned short u16x4 __attribute__((ext_vector_type(4)));

constexpr int H = 32, D = 128, MD = 512, DC = 512, SK = 8192, FF = 1376;
constexpr int NWG = 256, NT = 512;
constexpr int HG = 4;
constexpr int HPG = H / HG;
constexpr int NR = NWG / HG;
constexpr int KPR = SK / NR;
constexpr int NFC = 32;
constexpr int NFG = 43;
static_assert(HPG == 8 && KPR == 128, "geometry");

constexpr size_t WS_PART = 0;
constexpr size_t PART_BYTES = (size_t)H * 8 * NR * 64 * 4;
constexpr size_t WS_ML = WS_PART + PART_BYTES;
constexpr size_t ML_BYTES = (size_t)H * NR * 2 * 4;
constexpr size_t WS_FIN = WS_ML + ML_BYTES;
constexpr size_t FIN_BYTES = (size_t)NFC * NWG * 16 * 4;
constexpr size_t WS_QA = WS_FIN + FIN_BYTES;
constexpr size_t QA_BYTES = (size_t)H * DC * 2;
constexpr size_t WS_CTX = WS_QA + QA_BYTES;
constexpr size_t CTX_BYTES = (size_t)H * 8 * D * 4;
constexpr size_t ZERO_BYTES = WS_CTX + CTX_BYTES;
constexpr size_t WS_QP = ZERO_BYTES;
constexpr size_t QP_BYTES = (size_t)H * 8 * D * 4;
constexpr size_t WS_FP = WS_QP + QP_BYTES;
constexpr size_t FP_BYTES = (size_t)FF * 8 * 4;
constexpr size_t WS_END = WS_FP + FP_BYTES;
static_assert(ZERO_BYTES % (NWG * 16) == 0, "zero split");

constexpr unsigned SPIN_CAP = 1u << 20;
#define SLP 4
#define PRE_A 24
#define ZAUX 0
#define STAMP(k, i) do { } while (0)
#define STAMPW(k, i, tid) do { } while (0)

__device__ __forceinline__ float asf(unsigned u) { return __builtin_bit_cast(float, u); }
__device__ __forceinline__ unsigned asu(float f) { return __builtin_bit_cast(unsigned, f); }
__device__ __forceinline__ float bf_lo(unsigned u) { return __builtin_bit_cast(float, u << 16); }
__device__ __forceinline__ float bf_hi(unsigned u) { return __builtin_bit_cast(float, u & 0xffff0000u); }
__device__ __forceinline__ float bf2f(u16 v) { return __builtin_bit_cast(float, (unsigned)v << 16); }
__device__ __forceinline__ u16 f2bf(float f) {
  __bf16 t = (__bf16)f;
  return __builtin_bit_cast(u16, t);
}
__device__ __forceinline__ unsigned pack2(float a, float b) { return (unsigned)f2bf(a) | ((unsigned)f2bf(b) << 16); }
__device__ __forceinline__ unsigned tagf(float f) { return __builtin_bit_cast(unsigned, f) | 1u; }

__device__ __forceinline__ void unpack8(const v4u& v, float* o) {
  o[0] = bf_lo(v.x); o[1] = bf_hi(v.x); o[2] = bf_lo(v.y); o[3] = bf_hi(v.y);
  o[4] = bf_lo(v.z); o[5] = bf_hi(v.z); o[6] = bf_lo(v.w); o[7] = bf_hi(v.w);
}

template <int CTRL> __device__ __forceinline__ float dppf(float v) {
  return __builtin_bit_cast(float, __builtin_amdgcn_update_dpp(0, __builtin_bit_cast(int, v), CTRL, 0xf, 0xf, false));
}
__device__ __forceinline__ float row_sum(float v) {
  v += dppf<0x128>(v); v += dppf<0x124>(v); v += dppf<0x122>(v); v += dppf<0x121>(v);
  return v;
}
__device__ __forceinline__ float row_max(float v) {
  v = fmaxf(v, dppf<0x128>(v)); v = fmaxf(v, dppf<0x124>(v)); v = fmaxf(v, dppf<0x122>(v)); v = fmaxf(v, dppf<0x121>(v));
  return v;
}
__device__ __forceinline__ float row_sum_s4(float v) {
  v += dppf<0x128>(v); v += dppf<0x124>(v);
  return v;
}
__device__ __forceinline__ float rl(float v, int l) {
  return __builtin_bit_cast(float, __builtin_amdgcn_readlane(__builtin_bit_cast(int, v), l));
}
__device__ __forceinline__ float wave_sum(float v) {
  v = row_sum(v);
  return (rl(v, 0) + rl(v, 16)) + (rl(v, 32) + rl(v, 48));
}
__device__ __forceinline__ float wave_max(float v) {
  v = row_max(v);
  return fmaxf(fmaxf(rl(v, 0), rl(v, 16)), fmaxf(rl(v, 32), rl(v, 48)));
}
__device__ __forceinline__ float xrow_sum(float v) {
  auto a = __builtin_amdgcn_permlane16_swap(asu(v), asu(v), false, false);
  v = asf(a[0]) + asf(a[1]);
  auto b = __builtin_amdgcn_permlane32_swap(asu(v), asu(v), false, false);
  return asf(b[0]) + asf(b[1]);
}
__device__ __forceinline__ float xrow_max(float v) {
  auto a = __builtin_amdgcn_permlane16_swap(asu(v), asu(v), false, false);
  v = fmaxf(asf(a[0]), asf(a[1]));
  auto b = __builtin_amdgcn_permlane32_swap(asu(v), asu(v), false, false);
  return fmaxf(asf(b[0]), asf(b[1]));
}
__device__ __forceinline__ void glds16(const void* gsrc, unsigned lds_dst) {
  unsigned keep;
  asm volatile("s_mov_b32 %0, m0\n\ts_mov_b32 m0, %2\n\ts_nop 0\n\tglobal_load_lds_dwordx4 %1, off\n\ts_mov_b32 m0, %0"
               : "=&s"(keep) : "v"(gsrc), "s"(lds_dst) : "memory");
}

#define WG_BARRIER() do { asm volatile("s_waitcnt lgkmcnt(0)" ::: "memory"); __builtin_amdgcn_s_barrier(); asm volatile("" ::: "memory"); } while (0)

__global__ __launch_bounds__(NT, 2) void k1_q(const u16* __restrict__ x, const u16* __restrict__ w_q,
                                              const u16* __restrict__ w_gate, const u16* __restrict__ w_up,
                                              unsigned char* __restrict__ ws) {
  __shared__ __attribute__((aligned(16))) float qred[8][D];
  __shared__ __attribute__((aligned(16))) float fred[8][64];
  const int t = threadIdx.x, lane = t & 63, w = t >> 6;
  const int b = blockIdx.x;
  const int h = b >> 3, s = b & 7;
  const int rg = t >> 4, cg = t & 15;
  STAMP(0, 0);

  v4u wq[2];
#pragma unroll
  for (int i = 0; i < 2; ++i) wq[i] = *(const v4u*)(w_q + ((size_t)(s * 64 + 2 * rg + i) * H + h) * D + cg * 8);
  const unsigned xq = *(const unsigned*)(x + s * 64 + 2 * rg);
  const bool has_ffn = b < 4 * NFG;
  const int fg = has_ffn ? b % NFG : 0, ms = has_ffn ? b / NFG : 0;
  const int mf = ms * 128 + (t >> 2);
  const v4u wg = *(const v4u*)(w_gate + (size_t)mf * FF + fg * 32 + (t & 3) * 8);
  const v4u wu = *(const v4u*)(w_up + (size_t)mf * FF + fg * 32 + (t & 3) * 8);
  const float xf = bf2f(x[mf]);
  {
    v4u z = {0u, 0u, 0u, 0u};
    constexpr size_t per = ZERO_BYTES / NWG;
    const __amdgpu_buffer_rsrc_t rsz = __builtin_amdgcn_make_buffer_rsrc((void*)ws, 0, (int)ZERO_BYTES, 0x00020000);
    for (unsigned o = t * 16; o < per; o += NT * 16) __builtin_amdgcn_raw_buffer_store_b128(z, rsz, (int)(b * per + o), 0, ZAUX);
  }
  STAMP(0, 1);
  {
    float f0[8], f1[8];
    unpack8(wq[0], f0);
    unpack8(wq[1], f1);
    const float x0 = bf_lo(xq), x1 = bf_hi(xq);
    float acc[8];
#pragma unroll
    for (int j = 0; j < 8; ++j) acc[j] = xrow_sum(x0 * f0[j] + x1 * f1[j]);
    if (lane < 16) {
      *(f32x4*)&qred[w][lane * 8] = f32x4{acc[0], acc[1], acc[2], acc[3]};
      *(f32x4*)&qred[w][lane * 8 + 4] = f32x4{acc[4], acc[5], acc[6], acc[7]};
    }
  }
  if (has_ffn) {
    float fgv[8], fuv[8];
    unpack8(wg, fgv);
    unpack8(wu, fuv);
#pragma unroll
    for (int j = 0; j < 8; ++j) { fgv[j] = xrow_sum(row_sum_s4(fgv[j] * xf)); fuv[j] = xrow_sum(row_sum_s4(fuv[j] * xf)); }
    if (lane < 4) {
      *(f32x4*)&fred[w][lane * 8] = f32x4{fgv[0], fgv[1], fgv[2], fgv[3]};
      *(f32x4*)&fred[w][lane * 8 + 4] = f32x4{fgv[4], fgv[5], fgv[6], fgv[7]};
      *(f32x4*)&fred[w][32 + lane * 8] = f32x4{fuv[0], fuv[1], fuv[2], fuv[3]};
      *(f32x4*)&fred[w][32 + lane * 8 + 4] = f32x4{fuv[4], fuv[5], fuv[6], fuv[7]};
    }
  }
  WG_BARRIER();
  STAMP(0, 2);
  if (t < D) {
    float v = 0.f;
#pragma unroll
    for (int i = 0; i < 8; ++i) v += qred[i][t];
    ((float*)(ws + WS_QP))[(h * 8 + s) * D + t] = v;
  } else if (has_ffn && t < D + 64) {
    const int u = t - D;
    float a = 0.f;
#pragma unroll
    for (int i = 0; i < 8; ++i) a += fred[i][u];
    ((float*)(ws + WS_FP))[(fg * 32 + (u & 31)) * 8 + (u >> 5) * 4 + ms] = a;
  }
  STAMP(0, 3);
}

__device__ __forceinline__ unsigned swz(unsigned row) { return ((row & 3) << 2) | ((row >> 2) & 3); }
__device__ __forceinline__ unsigned lds_off(unsigned row, unsigned ch) { return 1024u * row + 16u * (ch ^ swz(row)); }

constexpr int PSTR = 136;
constexpr int QSTR = 520;

__global__ __launch_bounds__(NT + 256, 3) void k2_attn(const u16* __restrict__ x, const u16* __restrict__ c_kv,
                                                 const u16* __restrict__ w_uk, const u16* __restrict__ w_uv,
                                                 const u16* __restrict__ w_o, const u16* __restrict__ w_down,
                                                 const u16* __restrict__ gate_w, unsigned char* __restrict__ ws,
                                                 u16* __restrict__ out) {
  __shared__ __attribute__((aligned(1024))) unsigned char ctile[KPR * 1024];
  __shared__ __attribute__((aligned(16))) u16 qb[HPG * QSTR];
  __shared__ __attribute__((aligned(16))) u16 pl[HPG * PSTR];
  __shared__ __attribute__((aligned(16))) float red2[8 * 128];
  __shared__ __attribute__((aligned(16))) float red1[8 * 64];
  __shared__ __attribute__((aligned(16))) float wmax[HPG * 8];
  __shared__ __attribute__((aligned(16))) float wsum[HPG * 8];
  __shared__ __attribute__((aligned(16))) float small[8 * 16 + 8];

  const int t = (threadIdx.x + NT) % (NT + 256), lane = t & 63, w = t >> 6;
  const int b = blockIdx.x;
  const int xcd = b & 7, idx = b >> 3;
  const int r = xcd * 8 + (idx >> 2), g = idx & 3;
  const int ch_ = xcd * 4 + (idx & 3), cs = idx >> 2;
  const int rg = t >> 4, cg = t & 15;

  const __amdgpu_buffer_rsrc_t rsw = __builtin_amdgcn_make_buffer_rsrc((void*)ws, 0, (int)WS_END, 0x00020000);

  STAMPW(1, 0, 256);
  if (w < 8) {
    const unsigned cbase = __builtin_amdgcn_readfirstlane((unsigned)(size_t)ctile + w * 16384);
#pragma unroll
    for (int i = 0; i < 16; ++i) {
      const unsigned row = w * 16 + i;
      glds16(c_kv + ((size_t)r * KPR + row) * DC + (lane ^ swz(i)) * 8, cbase + i * 1024);
    }
    STAMPW(1, 12, 256);
    asm volatile("s_waitcnt vmcnt(0)" ::: "memory");
    STAMPW(1, 11, 256);
  } else {
    const int hr = (t - NT) >> 4;
    v2u qp[8];
#pragma unroll
    for (int i = 0; i < 8; ++i) qp[i] = *(const v2u*)(ws + WS_QP + ((size_t)(ch_ * 8 + i) * D + lane * 2) * 4);
    v4u wk[4];
#pragma unroll
    for (int i = 0; i < 4; ++i) wk[i] = *(const v4u*)(w_uk + ((size_t)(cs * 64 + 4 * hr + i) * H + ch_) * D + cg * 8);
    float qx = ((asf(qp[0].x) + asf(qp[1].x)) + (asf(qp[2].x) + asf(qp[3].x))) + ((asf(qp[4].x) + asf(qp[5].x)) + (asf(qp[6].x) + asf(qp[7].x)));
    float qy = ((asf(qp[0].y) + asf(qp[1].y)) + (asf(qp[2].y) + asf(qp[3].y))) + ((asf(qp[4].y) + asf(qp[5].y)) + (asf(qp[6].y) + asf(qp[7].y)));
    float* qw = red2 + (w - 8) * D;
    *(v2u*)&qw[lane * 2] = v2u{asu(qx), asu(qy)};
    asm volatile("s_waitcnt lgkmcnt(0)" ::: "memory");
    const float scale = 0.08838834764831845f * 1.4426950408889634f;
    const f32x4 q0 = *(const f32x4*)&qw[cg * 8], q1 = *(const f32x4*)&qw[cg * 8 + 4];
    float v[4];
#pragma unroll
    for (int i = 0; i < 4; ++i) {
      float f[8];
      unpack8(wk[i], f);
      float u = (q0[0] * f[0] + q0[1] * f[1]) + (q0[2] * f[2] + q0[3] * f[3]) + (q1[0] * f[4] + q1[1] * f[5]) + (q1[2] * f[6] + q1[3] * f[7]);
      v[i] = row_sum(u) * scale;
    }
    if (cg == 0) {
      const v2u o = {pack2(v[0], v[1]) | 1u, pack2(v[2], v[3]) | 1u};
      __builtin_amdgcn_raw_buffer_store_b64(o, rsw, (int)(WS_QA + (ch_ * DC + cs * 64 + 4 * hr) * 2), 0, 16);
    }
    STAMPW(1, 1, 0);
    __builtin_amdgcn_s_sleep(PRE_A);
    v4u qv[2];
    unsigned spins = 0;
    int zoff = 0;
    for (;;) {
      asm volatile("" : "+v"(zoff));
      unsigned all = 1u;
#pragma unroll
      for (int i = 0; i < 2; ++i) {
        qv[i] = __builtin_amdgcn_raw_buffer_load_b128(rsw, zoff + (int)(WS_QA + g * HPG * DC * 2 + (i * 256 + t - NT) * 16), 0, 16);
        all &= qv[i].x & qv[i].y & qv[i].z & qv[i].w;
      }
      if (__all((all & 1u) != 0)) break;
      if (++spins > SPIN_CAP) break;
      __builtin_amdgcn_s_sleep(SLP);
    }
    STAMPW(1, 10, 0);
#pragma unroll
    for (int i = 0; i < 2; ++i) *(v4u*)(qb + (i * 4 + w - 8) * QSTR + lane * 8) = qv[i];
    WG_BARRIER();
    return;
  }
  v4u wuv[2];
#pragma unroll
  for (int i = 0; i < 2; ++i) wuv[i] = *(const v4u*)(w_uv + ((size_t)(cs * 64 + rg + 32 * i) * H + ch_) * D + cg * 8);
  v4u wo[2];
#pragma unroll
  for (int i = 0; i < 2; ++i) wo[i] = *(const v4u*)(w_o + ((size_t)ch_ * D + cs * 16 + (lane >> 3) * 2 + i) * MD + w * 64 + (lane & 7) * 8);
  const bool has_ffn = b < FF / 8;
  const int fb = has_ffn ? b : 0;
  const v4u wd = *(const v4u*)(w_down + ((size_t)fb * 8 + (lane >> 3)) * MD + w * 64 + (lane & 7) * 8);
  const f32x4 fpg = *(const f32x4*)(ws + WS_FP + ((size_t)fb * 8 + (lane >> 3)) * 32);
  const f32x4 fpu = *(const f32x4*)(ws + WS_FP + ((size_t)fb * 8 + (lane >> 3)) * 32 + 16);
  const u16 x_raw = x[t], gw_raw = gate_w[t];
  WG_BARRIER();
  STAMPW(1, 2, 256);

  f32x4 sacc = {0.f, 0.f, 0.f, 0.f};
#pragma unroll
  for (int i = 0; i < 16; ++i) {
    const v4u afr = *(const v4u*)(ctile + lds_off(w * 16 + (lane & 15), i * 4 + (lane >> 4)));
    const v4u bfr = *(const v4u*)(qb + (lane & 7) * QSTR + i * 32 + (lane >> 4) * 8);
    sacc = __builtin_amdgcn_mfma_f32_16x16x32_bf16(__builtin_bit_cast(bf16x8, afr), __builtin_bit_cast(bf16x8, bfr), sacc, 0, 0, 0);
  }
  {
    const float mx = xrow_max(fmaxf(fmaxf(sacc[0], sacc[1]), fmaxf(sacc[2], sacc[3])));
    if (lane < 8) wmax[lane * 8 + w] = mx;
  }
  WG_BARRIER();
  float mref;
  {
    const f32x4 m0 = *(const f32x4*)&wmax[(lane & 7) * 8], m1 = *(const f32x4*)&wmax[(lane & 7) * 8 + 4];
    const float mx = fmaxf(fmaxf(fmaxf(m0[0], m0[1]), fmaxf(m0[2], m0[3])), fmaxf(fmaxf(m1[0], m1[1]), fmaxf(m1[2], m1[3])));
    mref = asf(tagf(mx));
    const unsigned p01 = pack2(__builtin_amdgcn_exp2f(sacc[0] - mref), __builtin_amdgcn_exp2f(sacc[1] - mref));
    const unsigned p23 = pack2(__builtin_amdgcn_exp2f(sacc[2] - mref), __builtin_amdgcn_exp2f(sacc[3] - mref));
    const float l = xrow_sum((bf_lo(p01) + bf_hi(p01)) + (bf_lo(p23) + bf_hi(p23)));
    if ((lane & 15) < 8) {
      *(v2u*)(pl + (lane & 7) * PSTR + w * 16 + (lane >> 4) * 4) = v2u{p01, p23};
      if (lane < 8) wsum[lane * 8 + w] = l;
    }
  }
  WG_BARRIER();
  {
    v4u pf[4];
#pragma unroll
    for (int ks = 0; ks < 4; ++ks) pf[ks] = *(const v4u*)(pl + (lane & 7) * PSTR + ks * 32 + (lane >> 4) * 8);
    if (w == 0 && lane < 8) {
      const f32x4 s0 = *(const f32x4*)&wsum[lane * 8], s1 = *(const f32x4*)&wsum[lane * 8 + 4];
      const float l = ((s0[0] + s0[1]) + (s0[2] + s0[3])) + ((s1[0] + s1[1]) + (s1[2] + s1[3]));
      const v2u o = {asu(mref), tagf(l)};
      __builtin_amdgcn_raw_buffer_store_b64(o, rsw, (int)(WS_ML + ((size_t)(g * HPG + lane) * NR + r) * 8), 0, 16);
    }
    const unsigned cbase = (unsigned)(size_t)ctile;
    const unsigned gq = lane >> 4, q = (lane & 15) >> 2, p = lane & 3;
#pragma unroll
    for (int nt = 0; nt < 4; ++nt) {
      u16x4 a[8];
      const unsigned cc = w * 4 + nt;
      const unsigned ad0 = cbase + lds_off(8 * gq + q, 2 * cc + (p >> 1)) + 8 * (p & 1);
      const unsigned ad1 = cbase + lds_off(8 * gq + 4 + q, 2 * cc + (p >> 1)) + 8 * (p & 1);
      const unsigned ad2 = ad0 + 65536u, ad3 = ad1 + 65536u;
      asm volatile("ds_read_b64_tr_b16 %0, %8\n\tds_read_b64_tr_b16 %1, %9\n\tds_read_b64_tr_b16 %2, %8 offset:32768\n\tds_read_b64_tr_b16 %3, %9 offset:32768\n\t"
                   "ds_read_b64_tr_b16 %4, %10\n\tds_read_b64_tr_b16 %5, %11\n\tds_read_b64_tr_b16 %6, %10 offset:32768\n\tds_read_b64_tr_b16 %7, %11 offset:32768\n\t"
                   "s_waitcnt lgkmcnt(0)"
                   : "=&v"(a[0]), "=&v"(a[1]), "=&v"(a[2]), "=&v"(a[3]), "=&v"(a[4]), "=&v"(a[5]), "=&v"(a[6]), "=&v"(a[7])
                   : "v"(ad0), "v"(ad1), "v"(ad2), "v"(ad3) : "memory");
      f32x4 acc = {0.f, 0.f, 0.f, 0.f};
#pragma unroll
      for (int ks = 0; ks < 4; ++ks) {
        bf16x8 af;
        af[0] = a[2 * ks][0]; af[1] = a[2 * ks][1]; af[2] = a[2 * ks][2]; af[3] = a[2 * ks][3];
        af[4] = a[2 * ks + 1][0]; af[5] = a[2 * ks + 1][1]; af[6] = a[2 * ks + 1][2]; af[7] = a[2 * ks + 1][3];
        acc = __builtin_amdgcn_mfma_f32_16x16x32_bf16(af, __builtin_bit_cast(bf16x8, pf[ks]), acc, 0, 0, 0);
      }
      if ((lane & 15) < 8) {
        const size_t off = WS_PART + ((((size_t)(g * HPG + (lane & 15)) * 8 + w) * NR + r) * 64 + nt * 16 + (lane >> 4) * 4) * 4;
        const v4u o = {tagf(acc[0]), tagf(acc[1]), tagf(acc[2]), tagf(acc[3])};
        __builtin_amdgcn_raw_buffer_store_b128(o, rsw, (int)off, 0, 16);
      }
    }
  }
  STAMPW(1, 3, 256);
  {
    const float gp = wave_sum(bf2f(x_raw) * bf2f(gw_raw));
    if (lane == 0) small[128 + w] = gp;
  }

  {
    v4u pv[2];
    v2u mlw, mlo[2];
    unsigned spins = 0;
    int zoff = 0;
    for (;;) {
      asm volatile("" : "+v"(zoff));
      mlw = __builtin_amdgcn_raw_buffer_load_b64(rsw, zoff + (int)(WS_ML + ((size_t)ch_ * NR + lane) * 8), 0, 16);
      unsigned all = mlw.x & mlw.y;
#pragma unroll
      for (int i = 0; i < 2; ++i) {
        mlo[i] = __builtin_amdgcn_raw_buffer_load_b64(rsw, zoff + (int)(WS_ML + ((size_t)ch_ * NR + rg + 32 * i) * 8), 0, 16);
        pv[i] = __builtin_amdgcn_raw_buffer_load_b128(rsw, zoff + (int)(WS_PART + ((((size_t)ch_ * 8 + cs) * NR + rg + 32 * i) * 64 + cg * 4) * 4), 0, 16);
        all &= mlo[i].x & pv[i].x & pv[i].y & pv[i].z & pv[i].w;
      }
      if (__all((all & 1u) != 0)) break;
      if (++spins > SPIN_CAP) break;
      __builtin_amdgcn_s_sleep(SLP);
    }
    STAMPW(1, 4, 256);
    const float mm = asf(mlw.x), ll = asf(mlw.y);
    const float M = wave_max(mm);
    const float L = wave_sum(__builtin_amdgcn_exp2f(mm - M) * ll);
    const float inv = 1.f / L;
    f32x4 a4 = {0.f, 0.f, 0.f, 0.f};
#pragma unroll
    for (int i = 0; i < 2; ++i) {
      const float e = __builtin_amdgcn_exp2f(asf(mlo[i].x) - M) * inv;
      a4[0] += e * asf(pv[i].x);
      a4[1] += e * asf(pv[i].y);
      a4[2] += e * asf(pv[i].z);
      a4[3] += e * asf(pv[i].w);
    }
#pragma unroll
    for (int j = 0; j < 4; ++j) a4[j] = xrow_sum(a4[j]);
    if (lane < 16) *(f32x4*)&red1[w * 64 + lane * 4] = a4;
    WG_BARRIER();
  }
  {
    float l0 = 0.f, l1 = 0.f;
#pragma unroll
    for (int i = 0; i < 8; ++i) { l0 += red1[i * 64 + rg]; l1 += red1[i * 64 + rg + 32]; }
    float f0[8], f1[8];
    unpack8(wuv[0], f0);
    unpack8(wuv[1], f1);
    f32x4 o0, o1;
#pragma unroll
    for (int j = 0; j < 4; ++j) {
      o0[j] = xrow_sum(l0 * f0[j] + l1 * f1[j]);
      o1[j] = xrow_sum(l0 * f0[4 + j] + l1 * f1[4 + j]);
    }
    if (lane < 16) {
      *(f32x4*)&red2[w * D + lane * 8] = o0;
      *(f32x4*)&red2[w * D + lane * 8 + 4] = o1;
    }
    WG_BARRIER();
    if (t < 32) {
      f32x4 v = *(const f32x4*)&red2[t * 4];
#pragma unroll
      for (int i = 1; i < 8; ++i) v += *(const f32x4*)&red2[i * D + t * 4];
      const v4u o = {tagf(v[0]), tagf(v[1]), tagf(v[2]), tagf(v[3])};
      __builtin_amdgcn_raw_buffer_store_b128(o, rsw, (int)(WS_CTX + ((size_t)(ch_ * 8 + cs) * D + t * 4) * 4), 0, 16);
    }
  }
  STAMPW(1, 5, 256);
  {
    float gs = 0.f;
#pragma unroll
    for (int i = 0; i < 8; ++i) gs += small[128 + i];
    const float gatev = 1.f / (1.f + __expf(-gs));
    const float ga = (fpg[0] + fpg[1]) + (fpg[2] + fpg[3]);
    const float ua = (fpu[0] + fpu[1]) + (fpu[2] + fpu[3]);
    const float hv = ga / (1.f + __expf(-ga)) * ua;
    const float hh = has_ffn ? hv * (1.f - gatev) : 0.f;
    float a8[8];
    {
      float f[8];
      unpack8(wd, f);
#pragma unroll
      for (int j = 0; j < 8; ++j) a8[j] = hh * f[j];
    }
    v2u cv;
    unsigned spins = 0;
    int zoff = 0;
    for (;;) {
      asm volatile("" : "+v"(zoff));
      cv = __builtin_amdgcn_raw_buffer_load_b64(rsw, zoff + (int)(WS_CTX + ((size_t)(ch_ * 8 + (lane & 7)) * D + cs * 16 + (lane >> 3) * 2) * 4), 0, 16);
      if (__all((cv.x & cv.y & 1u) != 0)) break;
      if (++spins > SPIN_CAP) break;
      __builtin_amdgcn_s_sleep(SLP);
    }
    STAMPW(1, 6, 256);
    float c0 = asf(cv.x), c1 = asf(cv.y);
    c0 += dppf<0xB1>(c0); c1 += dppf<0xB1>(c1);
    c0 += dppf<0x4E>(c0); c1 += dppf<0x4E>(c1);
    c0 += dppf<0x141>(c0); c1 += dppf<0x141>(c1);
    c0 *= gatev; c1 *= gatev;
    {
      float f0[8], f1[8];
      unpack8(wo[0], f0);
      unpack8(wo[1], f1);
#pragma unroll
      for (int j = 0; j < 8; ++j) a8[j] += c0 * f0[j] + c1 * f1[j];
    }
    float b4[4], b2[2];
#pragma unroll
    for (int j = 0; j < 4; ++j) {
      auto sw = __builtin_amdgcn_permlane32_swap(asu(a8[j]), asu(a8[j + 4]), false, false);
      b4[j] = asf(sw[0]) + asf(sw[1]);
    }
#pragma unroll
    for (int j = 0; j < 2; ++j) {
      auto sw = __builtin_amdgcn_permlane16_swap(asu(b4[j]), asu(b4[j + 2]), false, false);
      b2[j] = asf(sw[0]) + asf(sw[1]);
      b2[j] += dppf<0x128>(b2[j]);
    }
    if ((lane & 8) == 0) {
      const int m = w * 64 + (lane & 7) * 8 + (lane >> 5) * 4 + ((lane >> 4) & 1) * 2;
      const v2u o = {tagf(b2[0]), tagf(b2[1])};
      __builtin_amdgcn_raw_buffer_store_b64(o, rsw, (int)(WS_FIN + (((size_t)(m >> 4) * NWG + b) * 16 + (m & 15)) * 4), 0, 16);
    }
  }
  STAMPW(1, 7, 256);
  if (b < NFC) {
    v4u fv[2];
    unsigned spins = 0;
    int zoff = 0;
    for (;;) {
      asm volatile("" : "+v"(zoff));
      unsigned all = 1u;
#pragma unroll
      for (int i = 0; i < 2; ++i) {
        fv[i] = __builtin_amdgcn_raw_buffer_load_b128(rsw, zoff + (int)(WS_FIN + ((size_t)b * NWG * 16 + (i * NT + t) * 4) * 4), 0, 16);
        all &= fv[i].x & fv[i].y & fv[i].z & fv[i].w;
      }
      if (__all((all & 1u) != 0)) break;
      if (++spins > SPIN_CAP) break;
      __builtin_amdgcn_s_sleep(SLP);
    }
    STAMPW(1, 8, 256);
    f32x4 v;
    v[0] = xrow_sum(row_sum_s4(asf(fv[0].x) + asf(fv[1].x)));
    v[1] = xrow_sum(row_sum_s4(asf(fv[0].y) + asf(fv[1].y)));
    v[2] = xrow_sum(row_sum_s4(asf(fv[0].z) + asf(fv[1].z)));
    v[3] = xrow_sum(row_sum_s4(asf(fv[0].w) + asf(fv[1].w)));
    if (lane < 4) *(f32x4*)&small[w * 16 + lane * 4] = v;
    WG_BARRIER();
    if (t < 8) {
      float o0 = 0.f, o1 = 0.f;
#pragma unroll
      for (int i = 0; i < 8; ++i) { o0 += small[i * 16 + 2 * t]; o1 += small[i * 16 + 2 * t + 1]; }
      *(unsigned*)(out + b * 16 + 2 * t) = pack2(o0, o1);
    }
    STAMPW(1, 9, 256);
  }
}

extern "C" void kernel_launch(void* const* d_in, const int* in_sizes, int n_in, void* d_out, int out_size,
                              void* d_ws, size_t ws_size, hipStream_t stream) {
  const u16* x = (const u16*)d_in[0];
  const u16* c_kv = (const u16*)d_in[1];
  const u16* w_q = (const u16*)d_in[2];
  const u16* w_uk = (const u16*)d_in[3];
  const u16* w_uv = (const u16*)d_in[4];
  const u16* w_o = (const u16*)d_in[5];
  const u16* w_gate = (const u16*)d_in[7];
  const u16* w_up = (const u16*)d_in[8];
  const u16* w_down = (const u16*)d_in[9];
  const u16* gate_w = (const u16*)d_in[10];
  unsigned char* ws = (unsigned char*)d_ws;
  k1_q<<<NWG, NT, 0, stream>>>(x, w_q, w_gate, w_up, ws);
  k2_attn<<<NWG, NT + 256, 0, stream>>>(x, c_kv, w_uk, w_uv, w_o, w_down, gate_w, ws, (u16*)d_out);
}
```

```cpp
#include <hip/hip_runtime.h>
#include <hip/hip_bf16.h>

typedef unsigned short u16;
typedef unsigned v4u __attribute__((ext_vector_type(4)));
typedef unsigned v2u __attribute__((ext_vector_type(2)));
typedef float f32x4 __attribute__((ext_vector_type(4)));
typedef short bf16x8 __attribute__((ext_vector_type(8)));
typedef unsigned short u16x4 __attribute__((ext_vector_type(4)));

constexpr int H = 32, D = 128, MD = 512, DC = 512, SK = 8192, FF = 1376;
constexpr int NWG = 256, NT = 512;
constexpr int HG = 4;
constexpr int HPG = H / HG;
constexpr int NR = NWG / HG;
constexpr int KPR = SK / NR;
constexpr int NFC = 32;
constexpr int NFG = 43;
static_assert(HPG == 8 && KPR == 128, "geometry");

constexpr size_t WS_PART = 0;
constexpr size_t PART_BYTES = (size_t)H * 8 * NR * 64 * 4;
constexpr size_t WS_ML = WS_PART + PART_BYTES;
constexpr size_t ML_BYTES = (size_t)H * NR * 16;
constexpr size_t WS_FIN = WS_ML + ML_BYTES;
constexpr size_t FIN_BYTES = (size_t)NFC * NWG * 16 * 4;
constexpr size_t WS_CTX = WS_FIN + FIN_BYTES;
constexpr size_t CTX_BYTES = (size_t)H * 8 * D * 4;
constexpr size_t WS_FP = WS_CTX + CTX_BYTES;
constexpr size_t FP_BYTES = (size_t)(FF / 2) * 16 * 16;
constexpr size_t WS_QP = WS_FP + FP_BYTES;
constexpr size_t QP_BYTES = (size_t)H * 8 * D * 4;
constexpr size_t WS_QA = WS_QP + QP_BYTES;
constexpr size_t QA_BYTES = (size_t)H * DC * 4;
constexpr size_t WS_END = WS_QA + QA_BYTES;
constexpr int NTH = NT + 256;

constexpr unsigned SPIN_CAP = 1u << 20;
#define ALLV(c) __all(c)
#define SLP 4
#define PRE_A 32
#define PRE_Q 24
#define PRE_G 16
#define W0 256
#define B1 8
#define ZAUX 0
#define STAMP(k, i) do { } while (0)
#define STAMPW(k, i, tid) do { } while (0)

__device__ __forceinline__ float asf(unsigned u) { return __builtin_bit_cast(float, u); }
__device__ __forceinline__ unsigned asu(float f) { return __builtin_bit_cast(unsigned, f); }
__device__ __forceinline__ float bf_lo(unsigned u) { return __builtin_bit_cast(float, u << 16); }
__device__ __forceinline__ float bf_hi(unsigned u) { return __builtin_bit_cast(float, u & 0xffff0000u); }
__device__ __forceinline__ float bf2f(u16 v) { return __builtin_bit_cast(float, (unsigned)v << 16); }
__device__ __forceinline__ u16 f2bf(float f) {
  __bf16 t = (__bf16)f;
  return __builtin_bit_cast(u16, t);
}
__device__ __forceinline__ unsigned pack2(float a, float b) { return (unsigned)f2bf(a) | ((unsigned)f2bf(b) << 16); }
__device__ __forceinline__ unsigned pack2t(float a, float b) { return (__builtin_bit_cast(unsigned, a) >> 16) | 1u | ((unsigned)f2bf(b) << 16); }
__device__ __forceinline__ unsigned bsel(unsigned m, unsigned a, unsigned b) {
  unsigned r;
  asm("v_bfi_b32 %0, %1, %2, %3" : "=v"(r) : "v"(m), "v"(a), "v"(b));
  return r;
}
extern "C" __device__ unsigned long long llvm_dispatch_id() __asm("llvm.amdgcn.dispatch.id");
__device__ __forceinline__ unsigned r20(float f) { return (__builtin_bit_cast(unsigned, f) + 0x800u) & 0xfffff000u; }
struct Tag { unsigned t0, t1, t2, t3; };
__device__ __forceinline__ v4u enc(const Tag& T, float a, float b, float c, float d) {
  return v4u{r20(a) | T.t0, r20(b) | T.t1, r20(c) | T.t2, r20(d) | T.t3};
}
__device__ __forceinline__ unsigned dec(const Tag& T, v4u& v) {
  v.x ^= T.t0; v.y ^= T.t1; v.z ^= T.t2; v.w ^= T.t3;
  return (v.x | v.y) | (v.z | v.w);
}
#define FRESH(bad) __all(((bad) & 0xfffu) == 0u)

__device__ __forceinline__ void unpack8(const v4u& v, float* o) {
  o[0] = bf_lo(v.x); o[1] = bf_hi(v.x); o[2] = bf_lo(v.y); o[3] = bf_hi(v.y);
  o[4] = bf_lo(v.z); o[5] = bf_hi(v.z); o[6] = bf_lo(v.w); o[7] = bf_hi(v.w);
}

template <int CTRL> __device__ __forceinline__ float dppf(float v) {
  return __builtin_bit_cast(float, __builtin_amdgcn_update_dpp(0, __builtin_bit_cast(int, v), CTRL, 0xf, 0xf, false));
}
__device__ __forceinline__ float row_sum(float v) {
  v += dppf<0x128>(v); v += dppf<0x124>(v); v += dppf<0x122>(v); v += dppf<0x121>(v);
  return v;
}
__device__ __forceinline__ float row_max(float v) {
  v = fmaxf(v, dppf<0x128>(v)); v = fmaxf(v, dppf<0x124>(v)); v = fmaxf(v, dppf<0x122>(v)); v = fmaxf(v, dppf<0x121>(v));
  return v;
}
__device__ __forceinline__ float row_sum_s4(float v) {
  v += dppf<0x128>(v); v += dppf<0x124>(v);
  return v;
}
__device__ __forceinline__ float rl(float v, int l) {
  return __builtin_bit_cast(float, __builtin_amdgcn_readlane(__builtin_bit_cast(int, v), l));
}
__device__ __forceinline__ float wave_sum(float v) {
  v = row_sum(v);
  return (rl(v, 0) + rl(v, 16)) + (rl(v, 32) + rl(v, 48));
}
__device__ __forceinline__ float wave_max(float v) {
  v = row_max(v);
  return fmaxf(fmaxf(rl(v, 0), rl(v, 16)), fmaxf(rl(v, 32), rl(v, 48)));
}
__device__ __forceinline__ float xrow_sum(float v) {
  auto a = __builtin_amdgcn_permlane16_swap(asu(v), asu(v), false, false);
  v = asf(a[0]) + asf(a[1]);
  auto b = __builtin_amdgcn_permlane32_swap(asu(v), asu(v), false, false);
  return asf(b[0]) + asf(b[1]);
}
__device__ __forceinline__ float xrow_max(float v) {
  auto a = __builtin_amdgcn_permlane16_swap(asu(v), asu(v), false, false);
  v = fmaxf(asf(a[0]), asf(a[1]));
  auto b = __builtin_amdgcn_permlane32_swap(asu(v), asu(v), false, false);
  return fmaxf(asf(b[0]), asf(b[1]));
}
#define NTW 0
#define NTC 0
#define NTSTR ""
__device__ __forceinline__ v4u ldw(const void* p) {
  return *(const v4u*)p;
}
__device__ __forceinline__ void glds16(const void* gsrc, unsigned lds_dst) {
  unsigned keep;
  asm volatile("s_mov_b32 %0, m0\n\ts_mov_b32 m0, %2\n\ts_nop 0\n\tglobal_load_lds_dwordx4 %1, off" NTSTR "\n\ts_mov_b32 m0, %0"
               : "=&s"(keep) : "v"(gsrc), "s"(lds_dst) : "memory");
}

__device__ __forceinline__ unsigned lds_rd(unsigned addr) {
  unsigned v;
  asm volatile("ds_read_b32 %0, %1\n\ts_waitcnt lgkmcnt(0)" : "=v"(v) : "v"(addr) : "memory");
  return __builtin_amdgcn_readfirstlane(v);
}
__device__ __forceinline__ void lds_wr(unsigned addr, unsigned v) {
  asm volatile("ds_write_b32 %0, %1\n\ts_waitcnt lgkmcnt(0)" :: "v"(addr), "v"(v) : "memory");
}

#define WG_BARRIER() do { asm volatile("s_waitcnt lgkmcnt(0)" ::: "memory"); __builtin_amdgcn_s_barrier(); asm volatile("" ::: "memory"); } while (0)

__device__ __forceinline__ void glds4(const void* gsrc, unsigned lds_dst) {
  unsigned keep;
  asm volatile("s_mov_b32 %0, m0\n\ts_mov_b32 m0, %2\n\ts_nop 0\n\tglobal_load_lds_dword %1, off sc1\n\ts_mov_b32 m0, %0"
               : "=&s"(keep) : "v"(gsrc), "s"(lds_dst) : "memory");
}
__device__ __forceinline__ unsigned swz(unsigned row) { return ((row & 3) << 2) | ((row >> 2) & 3); }
__device__ __forceinline__ unsigned lds_off(unsigned row, unsigned ch) { return 1024u * row + 16u * (ch ^ swz(row)); }

constexpr int PSTR = 136;
constexpr int QSTR = 520;

__global__ __launch_bounds__(NTH, 3) void k2_attn(const u16* __restrict__ x, const u16* __restrict__ c_kv,
                                                 const u16* __restrict__ w_q, const u16* __restrict__ w_gate,
                                                 const u16* __restrict__ w_up,
                                                 const u16* __restrict__ w_uk, const u16* __restrict__ w_uv,
                                                 const u16* __restrict__ w_o, const u16* __restrict__ w_down,
                                                 const u16* __restrict__ gate_w, unsigned char* __restrict__ ws,
                                                 u16* __restrict__ out) {
  __shared__ __attribute__((aligned(1024))) unsigned char ctile[KPR * 1024];
  __shared__ __attribute__((aligned(16))) u16 qb[HPG * QSTR];
  __shared__ __attribute__((aligned(16))) u16 pl[HPG * PSTR];
  __shared__ __attribute__((aligned(16))) float red2[8 * 128];
  __shared__ __attribute__((aligned(16))) float red1[8 * 64];
  __shared__ __attribute__((aligned(16))) float wmax[HPG * 8];
  __shared__ __attribute__((aligned(16))) float wsum[HPG * 8];
  __shared__ __attribute__((aligned(16))) float small[8 * 16 + 8];
  __shared__ __attribute__((aligned(16))) unsigned dummy[8 * 64];

  const int t = (threadIdx.x + NT) % NTH, lane = t & 63, w = t >> 6;
  const int b = blockIdx.x;
  const int xcd = b & 7, idx = b >> 3;
  const int r = xcd * 8 + (idx >> 2), g = idx & 3;
  const int ch_ = xcd * 4 + (idx & 3), cs = idx >> 2;
  const int rg = t >> 4, cg = t & 15;

  const __amdgpu_buffer_rsrc_t rsw = __builtin_amdgcn_make_buffer_rsrc((void*)ws, 0, (int)WS_END, 0x00020000);
  Tag T;
  {
    const unsigned long long M48 = (1ull << 48) - 1;
    unsigned long long u = (llvm_dispatch_id() & 0xffffffull) | ((((unsigned long long)__builtin_amdgcn_dispatch_ptr() >> 12) & 0xffffffull) << 24);
    u = (u + 0x5851f42d4c95ull) & M48;
    u ^= u >> 23; u = (u * 0x2545f4914f6dull) & M48;
    u ^= u >> 25; u = (u * 0x9e3779b97f4bull) & M48;
    u ^= u >> 24;
    T.t0 = (unsigned)u & 0xfffu; T.t1 = (unsigned)(u >> 12) & 0xfffu; T.t2 = (unsigned)(u >> 24) & 0xfffu; T.t3 = (unsigned)(u >> 36) & 0xfffu;
  }

  v4u wuv[2], wo[2], wd;
  u16 x_raw, gw_raw;
  const bool has_ffn = b < FF / 8;
  const int fb = has_ffn ? b : 0;
  STAMPW(0, 0, 0); STAMPW(1, 0, W0);
  if (w < 8) {
    const unsigned cbase = __builtin_amdgcn_readfirstlane((unsigned)(size_t)ctile + w * 16384);
#pragma unroll
    for (int i = 0; i < 16; ++i) {
      if (i == B1) {
#pragma unroll
        for (int k = 0; k < 2; ++k) wuv[k] = ldw(w_uv + ((size_t)(cs * 64 + rg + 32 * k) * H + ch_) * D + cg * 8);
        x_raw = x[t]; gw_raw = gate_w[t];
        {
          const int l16 = lane & 15;
          size_t po;
          if (lane < 16) po = WS_PART + ((size_t)ch_ * 8 + cs) * 16384 + (w * 16 + l16) * 128;
          else if (lane < 32) po = WS_FIN + (size_t)(b & (NFC - 1)) * 16384 + (w * 16 + l16) * 128;
          else if (lane < 36) po = WS_CTX + (size_t)ch_ * 4096 + (w * 4 + (lane & 3)) * 128;
          else po = WS_ML + (size_t)ch_ * 1024 + w * 128;
          if (lane < 37) glds4(ws + po, __builtin_amdgcn_readfirstlane((unsigned)(size_t)dummy + w * 256));
        }
        STAMPW(1, 18, W0); STAMPW(1, 19, W0 + 7 * 64);
        __builtin_amdgcn_s_barrier();
      }
      const unsigned row = w * 16 + i;
      glds16(c_kv + ((size_t)r * KPR + row) * DC + (lane ^ swz(i)) * 8, cbase + i * 1024);
    }
    STAMPW(1, 12, W0);
    asm volatile("s_waitcnt vmcnt(0)" ::: "memory");
    STAMPW(1, 11, W0); STAMPW(1, 16, W0 + 7 * 64); STAMPW(1, 17, W0 + 3 * 64);
  } else {
    const int hw = w - 8;
    const int hr = (t - NT) >> 4;
    v4u wq[4];
    u16 xr[4];
#pragma unroll
    for (int i = 0; i < 4; ++i) {
      const int row = cs * 64 + i * 16 + (lane >> 2);
      wq[i] = ldw(w_q + ((size_t)row * H + ch_) * D + hw * 32 + (lane & 3) * 8);
      xr[i] = x[row];
    }
    v4u wk[4];
#pragma unroll
    for (int i = 0; i < 4; ++i) wk[i] = ldw(w_uk + ((size_t)(cs * 64 + 4 * hr + i) * H + ch_) * D + cg * 8);
    {
      const size_t po = lane < 32 ? WS_QA + (size_t)g * 16384 + (hw * 32 + lane) * 128 : WS_QP + (size_t)ch_ * 4096 + (hw * 8 + (lane & 7)) * 128;
      if (lane < 40) glds4(ws + po, __builtin_amdgcn_readfirstlane((unsigned)(size_t)pl + hw * 256));
    }
    {
      float acc[8];
#pragma unroll
      for (int j = 0; j < 8; ++j) acc[j] = 0.f;
#pragma unroll
      for (int i = 0; i < 4; ++i) {
        float f[8];
        unpack8(wq[i], f);
        const float xv = bf2f(xr[i]);
#pragma unroll
        for (int j = 0; j < 8; ++j) acc[j] += xv * f[j];
      }
#pragma unroll
      for (int j = 0; j < 8; ++j) acc[j] = xrow_sum(row_sum_s4(acc[j]));
      const bool hi = (lane & 4) != 0;
      const v4u qo = enc(T, hi ? acc[4] : acc[0], hi ? acc[5] : acc[1], hi ? acc[6] : acc[2], hi ? acc[7] : acc[3]);
      if (lane < 8) __builtin_amdgcn_raw_buffer_store_b128(qo, rsw, (int)(WS_QP + ((ch_ * 8 + cs) * D + hw * 32 + (lane & 3) * 8 + (lane & 4)) * 4), 0, 16);
    }
    STAMPW(1, 20, 0);
    __builtin_amdgcn_s_sleep(PRE_Q);
    v4u qg[4];
    int zoff = 0;
    unsigned spins = 0;
    for (;;) {
      asm volatile("" : "+v"(zoff));
      unsigned bad = 0u;
#pragma unroll
      for (int i = 0; i < 4; ++i) {
        qg[i] = __builtin_amdgcn_raw_buffer_load_b128(rsw, zoff + (int)(WS_QP + ((ch_ * 8 + (lane >> 5) + 2 * i) * D + (lane & 31) * 4) * 4), 0, 16);
        bad |= dec(T, qg[i]);
      }
      if (FRESH(bad)) break;
      if (++spins > SPIN_CAP) break;
      __builtin_amdgcn_s_sleep(SLP);
    }
    STAMPW(1, 21, 0);
    float* qw = red2 + hw * D;
    {
      f32x4 q4;
      q4[0] = (asf(qg[0].x) + asf(qg[1].x)) + (asf(qg[2].x) + asf(qg[3].x));
      q4[1] = (asf(qg[0].y) + asf(qg[1].y)) + (asf(qg[2].y) + asf(qg[3].y));
      q4[2] = (asf(qg[0].z) + asf(qg[1].z)) + (asf(qg[2].z) + asf(qg[3].z));
      q4[3] = (asf(qg[0].w) + asf(qg[1].w)) + (asf(qg[2].w) + asf(qg[3].w));
#pragma unroll
      for (int j = 0; j < 4; ++j) {
        auto sw = __builtin_amdgcn_permlane32_swap(asu(q4[j]), asu(q4[j]), false, false);
        q4[j] = asf(sw[0]) + asf(sw[1]);
      }
      if (lane < 32) *(f32x4*)&qw[lane * 4] = q4;
    }
    asm volatile("s_waitcnt lgkmcnt(0)" ::: "memory");
    const float scale = 0.08838834764831845f * 1.4426950408889634f;
    const f32x4 q0 = *(const f32x4*)&qw[cg * 8], q1 = *(const f32x4*)&qw[cg * 8 + 4];
    float v[4];
#pragma unroll
    for (int i = 0; i < 4; ++i) {
      float f[8];
      unpack8(wk[i], f);
      float u = (q0[0] * f[0] + q0[1] * f[1]) + (q0[2] * f[2] + q0[3] * f[3]) + (q1[0] * f[4] + q1[1] * f[5]) + (q1[2] * f[6] + q1[3] * f[7]);
      v[i] = row_sum(u) * scale;
    }
    if (cg == 0) __builtin_amdgcn_raw_buffer_store_b128(enc(T, v[0], v[1], v[2], v[3]), rsw, (int)(WS_QA + (ch_ * DC + cs * 64 + 4 * hr) * 4), 0, 16);
    __builtin_amdgcn_s_barrier();
    STAMPW(1, 1, 0);
    const bool ffn_p = b < 4 * NFG;
    const int fg = ffn_p ? b % NFG : 0, fk = (ffn_p ? b / NFG : 0) * 4 + hw;
    v4u wg[2], wu[2];
    u16 xf[2];
#pragma unroll
    for (int i = 0; i < 2; ++i) {
      const int row = fk * 32 + i * 16 + (lane >> 2);
      wg[i] = ldw(w_gate + (size_t)row * FF + fg * 32 + (lane & 3) * 8);
      wu[i] = ldw(w_up + (size_t)row * FF + fg * 32 + (lane & 3) * 8);
      xf[i] = x[row];
    }
    __builtin_amdgcn_s_sleep(PRE_A);
    spins = 0;
    for (;;) {
      asm volatile("" : "+v"(zoff));
      unsigned bad = 0u;
#pragma unroll
      for (int i = 0; i < 4; ++i) {
        qg[i] = __builtin_amdgcn_raw_buffer_load_b128(rsw, zoff + (int)(WS_QA + g * HPG * DC * 4 + (i * 256 + t - NT) * 16), 0, 16);
        bad |= dec(T, qg[i]);
      }
      if (FRESH(bad)) break;
      if (++spins > SPIN_CAP) break;
      __builtin_amdgcn_s_sleep(SLP);
    }
    STAMPW(1, 10, 0); STAMPW(1, 13, 64); STAMPW(1, 14, 128); STAMPW(1, 15, 192);
#pragma unroll
    for (int i = 0; i < 4; ++i)
      *(v2u*)(qb + (i * 2 + (hw >> 1)) * QSTR + ((hw & 1) * 64 + lane) * 4) = v2u{pack2(asf(qg[i].x), asf(qg[i].y)), pack2(asf(qg[i].z), asf(qg[i].w))};
    WG_BARRIER();
    asm volatile("" : "+v"(wg[0]), "+v"(wg[1]), "+v"(wu[0]), "+v"(wu[1]));
    {
      float g0[8], g1[8], u0[8], u1[8];
      unpack8(wg[0], g0); unpack8(wg[1], g1); unpack8(wu[0], u0); unpack8(wu[1], u1);
      const float x0 = bf2f(xf[0]), x1 = bf2f(xf[1]);
      unsigned gs[8], us[8];
#pragma unroll
      for (int j = 0; j < 8; ++j) {
        gs[j] = asu(xrow_sum(row_sum_s4(x0 * g0[j] + x1 * g1[j])));
        us[j] = asu(xrow_sum(row_sum_s4(x0 * u0[j] + x1 * u1[j])));
      }
      const unsigned m8 = (lane & 8) ? ~0u : 0u, m4 = (lane & 4) ? ~0u : 0u;
      const unsigned e0 = bsel(m4, bsel(m8, gs[6], gs[2]), bsel(m8, gs[4], gs[0]));
      const unsigned e1 = bsel(m4, bsel(m8, us[6], us[2]), bsel(m8, us[4], us[0]));
      const unsigned e2 = bsel(m4, bsel(m8, gs[7], gs[3]), bsel(m8, gs[5], gs[1]));
      const unsigned e3 = bsel(m4, bsel(m8, us[7], us[3]), bsel(m8, us[5], us[1]));
      if (ffn_p && lane < 16)
        __builtin_amdgcn_raw_buffer_store_b128(enc(T, asf(e0), asf(e1), asf(e2), asf(e3)), rsw,
                                               (int)(WS_FP + ((fg * 16 + (lane & 3) * 4 + (lane >> 2)) * 16 + fk) * 16), 0, 16);
    }
    STAMPW(1, 24, 0);
    return;
  }
  WG_BARRIER();
  STAMPW(1, 2, W0);

  f32x4 sacc = {0.f, 0.f, 0.f, 0.f};
#pragma unroll
  for (int i = 0; i < 16; ++i) {
    const v4u afr = *(const v4u*)(ctile + lds_off(w * 16 + (lane & 15), i * 4 + (lane >> 4)));
    const v4u bfr = *(const v4u*)(qb + (lane & 7) * QSTR + i * 32 + (lane >> 4) * 8);
    sacc = __builtin_amdgcn_mfma_f32_16x16x32_bf16(__builtin_bit_cast(bf16x8, afr), __builtin_bit_cast(bf16x8, bfr), sacc, 0, 0, 0);
  }
  {
    const float mx = xrow_max(fmaxf(fmaxf(sacc[0], sacc[1]), fmaxf(sacc[2], sacc[3])));
    if (lane < 8) wmax[lane * 8 + w] = mx;
  }
  WG_BARRIER();
  float mref;
  {
    const f32x4 m0 = *(const f32x4*)&wmax[(lane & 7) * 8], m1 = *(const f32x4*)&wmax[(lane & 7) * 8 + 4];
    const float mx = fmaxf(fmaxf(fmaxf(m0[0], m0[1]), fmaxf(m0[2], m0[3])), fmaxf(fmaxf(m1[0], m1[1]), fmaxf(m1[2], m1[3])));
    mref = asf(r20(mx));
    const unsigned p01 = pack2(__builtin_amdgcn_exp2f(sacc[0] - mref), __builtin_amdgcn_exp2f(sacc[1] - mref));
    const unsigned p23 = pack2(__builtin_amdgcn_exp2f(sacc[2] - mref), __builtin_amdgcn_exp2f(sacc[3] - mref));
    const float l = xrow_sum((bf_lo(p01) + bf_hi(p01)) + (bf_lo(p23) + bf_hi(p23)));
    if ((lane & 15) < 8) {
      *(v2u*)(pl + (lane & 7) * PSTR + w * 16 + (lane >> 4) * 4) = v2u{p01, p23};
      if (lane < 8) wsum[lane * 8 + w] = l;
    }
  }
  WG_BARRIER();
  {
    v4u pf[4];
#pragma unroll
    for (int ks = 0; ks < 4; ++ks) pf[ks] = *(const v4u*)(pl + (lane & 7) * PSTR + ks * 32 + (lane >> 4) * 8);
    if (w == 0 && lane < 8) {
      const f32x4 s0 = *(const f32x4*)&wsum[lane * 8], s1 = *(const f32x4*)&wsum[lane * 8 + 4];
      const float l = ((s0[0] + s0[1]) + (s0[2] + s0[3])) + ((s1[0] + s1[1]) + (s1[2] + s1[3]));
      const float lh = asf(r20(l));
      __builtin_amdgcn_raw_buffer_store_b128(enc(T, mref, lh, l - lh, 0.f), rsw, (int)(WS_ML + ((size_t)(g * HPG + lane) * NR + r) * 16), 0, 16);
    }
    const unsigned cbase = (unsigned)(size_t)ctile;
    const unsigned gq = lane >> 4, q = (lane & 15) >> 2, p = lane & 3;
#pragma unroll
    for (int nt = 0; nt < 4; ++nt) {
      u16x4 a[8];
      const unsigned cc = w * 4 + nt;
      const unsigned ad0 = cbase + lds_off(8 * gq + q, 2 * cc + (p >> 1)) + 8 * (p & 1);
      const unsigned ad1 = cbase + lds_off(8 * gq + 4 + q, 2 * cc + (p >> 1)) + 8 * (p & 1);
      const unsigned ad2 = ad0 + 65536u, ad3 = ad1 + 65536u;
      asm volatile("ds_read_b64_tr_b16 %0, %8\n\tds_read_b64_tr_b16 %1, %9\n\tds_read_b64_tr_b16 %2, %8 offset:32768\n\tds_read_b64_tr_b16 %3, %9 offset:32768\n\t"
                   "ds_read_b64_tr_b16 %4, %10\n\tds_read_b64_tr_b16 %5, %11\n\tds_read_b64_tr_b16 %6, %10 offset:32768\n\tds_read_b64_tr_b16 %7, %11 offset:32768\n\t"
                   "s_waitcnt lgkmcnt(0)"
                   : "=&v"(a[0]), "=&v"(a[1]), "=&v"(a[2]), "=&v"(a[3]), "=&v"(a[4]), "=&v"(a[5]), "=&v"(a[6]), "=&v"(a[7])
                   : "v"(ad0), "v"(ad1), "v"(ad2), "v"(ad3) : "memory");
      f32x4 acc = {0.f, 0.f, 0.f, 0.f};
#pragma unroll
      for (int ks = 0; ks < 4; ++ks) {
        bf16x8 af;
        af[0] = a[2 * ks][0]; af[1] = a[2 * ks][1]; af[2] = a[2 * ks][2]; af[3] = a[2 * ks][3];
        af[4] = a[2 * ks + 1][0]; af[5] = a[2 * ks + 1][1]; af[6] = a[2 * ks + 1][2]; af[7] = a[2 * ks + 1][3];
        acc = __builtin_amdgcn_mfma_f32_16x16x32_bf16(af, __builtin_bit_cast(bf16x8, pf[ks]), acc, 0, 0, 0);
      }
      if ((lane & 15) < 8) {
        const size_t off = WS_PART + ((((size_t)(g * HPG + (lane & 15)) * 8 + w) * NR + r) * 64 + nt * 16 + (lane >> 4) * 4) * 4;
        __builtin_amdgcn_raw_buffer_store_b128(enc(T, acc[0], acc[1], acc[2], acc[3]), rsw, (int)off, 0, 16);
      }
    }
  }
  STAMPW(1, 3, W0);
#pragma unroll
  for (int k = 0; k < 2; ++k) wo[k] = ldw(w_o + ((size_t)ch_ * D + cs * 16 + (lane >> 3) * 2 + k) * MD + w * 64 + (lane & 7) * 8);
  wd = ldw(w_down + ((size_t)fb * 8 + (lane >> 3)) * MD + w * 64 + (lane & 7) * 8);
  {
    const float gp = wave_sum(bf2f(x_raw) * bf2f(gw_raw));
    if (lane == 0) small[128 + w] = gp;
  }

  v4u fpv;
  {
    v4u pv[2], mlw, mlo[2];
    unsigned spins = 0;
    int zoff = 0;
    for (;;) {
      asm volatile("" : "+v"(zoff));
      mlw = __builtin_amdgcn_raw_buffer_load_b128(rsw, zoff + (int)(WS_ML + ((size_t)ch_ * NR + lane) * 16), 0, 16);
      unsigned bad = dec(T, mlw);
#pragma unroll
      for (int i = 0; i < 2; ++i) {
        mlo[i] = __builtin_amdgcn_raw_buffer_load_b128(rsw, zoff + (int)(WS_ML + ((size_t)ch_ * NR + rg + 32 * i) * 16), 0, 16);
        pv[i] = __builtin_amdgcn_raw_buffer_load_b128(rsw, zoff + (int)(WS_PART + ((((size_t)ch_ * 8 + cs) * NR + rg + 32 * i) * 64 + cg * 4) * 4), 0, 16);
        bad |= dec(T, mlo[i]) | dec(T, pv[i]);
      }
      if (FRESH(bad)) break;
      if (++spins > SPIN_CAP) break;
      __builtin_amdgcn_s_sleep(SLP);
    }
    STAMPW(1, 4, W0);
    fpv = __builtin_amdgcn_raw_buffer_load_b128(rsw, (int)(WS_FP + (size_t)fb * 1024 + lane * 16), 0, 16);
    const float mm = asf(mlw.x), ll = asf(mlw.y) + asf(mlw.z);
    const float M = wave_max(mm);
    const float L = wave_sum(__builtin_amdgcn_exp2f(mm - M) * ll);
    const float inv = 1.f / L;
    f32x4 a4 = {0.f, 0.f, 0.f, 0.f};
#pragma unroll
    for (int i = 0; i < 2; ++i) {
      const float e = __builtin_amdgcn_exp2f(asf(mlo[i].x) - M) * inv;
      a4[0] += e * asf(pv[i].x);
      a4[1] += e * asf(pv[i].y);
      a4[2] += e * asf(pv[i].z);
      a4[3] += e * asf(pv[i].w);
    }
#pragma unroll
    for (int j = 0; j < 4; ++j) a4[j] = xrow_sum(a4[j]);
    if (lane < 16) *(f32x4*)&red1[w * 64 + lane * 4] = a4;
    WG_BARRIER();
  }
  {
    float l0 = 0.f, l1 = 0.f;
#pragma unroll
    for (int i = 0; i < 8; ++i) { l0 += red1[i * 64 + rg]; l1 += red1[i * 64 + rg + 32]; }
    float f0[8], f1[8];
    unpack8(wuv[0], f0);
    unpack8(wuv[1], f1);
    f32x4 o0, o1;
#pragma unroll
    for (int j = 0; j < 4; ++j) {
      o0[j] = xrow_sum(l0 * f0[j] + l1 * f1[j]);
      o1[j] = xrow_sum(l0 * f0[4 + j] + l1 * f1[4 + j]);
    }
    if (lane < 16) {
      *(f32x4*)&red2[w * D + lane * 8] = o0;
      *(f32x4*)&red2[w * D + lane * 8 + 4] = o1;
    }
    WG_BARRIER();
    if (t < 32) {
      f32x4 v = *(const f32x4*)&red2[t * 4];
#pragma unroll
      for (int i = 1; i < 8; ++i) v += *(const f32x4*)&red2[i * D + t * 4];
      __builtin_amdgcn_raw_buffer_store_b128(enc(T, v[0], v[1], v[2], v[3]), rsw, (int)(WS_CTX + ((size_t)(ch_ * 8 + cs) * D + t * 4) * 4), 0, 16);
    }
  }
  STAMPW(1, 5, W0);
  {
    float gs = 0.f;
#pragma unroll
    for (int i = 0; i < 8; ++i) gs += small[128 + i];
    const float gatev = 1.f / (1.f + __expf(-gs));
    {
      unsigned spins = 0;
      int zoff = 0;
      unsigned bad = dec(T, fpv);
      while (!FRESH(bad) && ++spins < SPIN_CAP) {
        asm volatile("" : "+v"(zoff));
        __builtin_amdgcn_s_sleep(SLP);
        fpv = __builtin_amdgcn_raw_buffer_load_b128(rsw, zoff + (int)(WS_FP + (size_t)fb * 1024 + lane * 16), 0, 16);
        bad = dec(T, fpv);
      }
    }
    const float g0 = row_sum(asf(fpv.x)), u0 = row_sum(asf(fpv.y)), g1 = row_sum(asf(fpv.z)), u1 = row_sum(asf(fpv.w));
    const float ga = (lane & 8) ? g1 : g0;
    const float ua = (lane & 8) ? u1 : u0;
    const float hv = ga / (1.f + __expf(-ga)) * ua;
    const float hh = has_ffn ? hv * (1.f - gatev) : 0.f;
    float a8[8];
    {
      float f[8];
      unpack8(wd, f);
#pragma unroll
      for (int j = 0; j < 8; ++j) a8[j] = hh * f[j];
    }
    v4u cv;
    unsigned spins = 0;
    int zoff = 0;
    for (;;) {
      asm volatile("" : "+v"(zoff));
      cv = __builtin_amdgcn_raw_buffer_load_b128(rsw, zoff + (int)(WS_CTX + ((size_t)(ch_ * 8 + (lane & 7)) * D + cs * 16 + (lane >> 4) * 4) * 4), 0, 16);
      if (FRESH(dec(T, cv))) break;
      if (++spins > SPIN_CAP) break;
      __builtin_amdgcn_s_sleep(SLP);
    }
    STAMPW(1, 6, W0);
    float c0 = asf((lane & 8) ? cv.z : cv.x), c1 = asf((lane & 8) ? cv.w : cv.y);
    c0 += dppf<0xB1>(c0); c1 += dppf<0xB1>(c1);
    c0 += dppf<0x4E>(c0); c1 += dppf<0x4E>(c1);
    c0 += dppf<0x141>(c0); c1 += dppf<0x141>(c1);
    c0 *= gatev; c1 *= gatev;
    {
      float f0[8], f1[8];
      unpack8(wo[0], f0);
      unpack8(wo[1], f1);
#pragma unroll
      for (int j = 0; j < 8; ++j) a8[j] += c0 * f0[j] + c1 * f1[j];
    }
    float b4[4];
#pragma unroll
    for (int j = 0; j < 4; ++j) {
      auto sw = __builtin_amdgcn_permlane32_swap(asu(a8[j]), asu(a8[j + 4]), false, false);
      b4[j] = asf(sw[0]) + asf(sw[1]);
      auto s2 = __builtin_amdgcn_permlane16_swap(asu(b4[j]), asu(b4[j]), false, false);
      b4[j] = asf(s2[0]) + asf(s2[1]);
      b4[j] += dppf<0x128>(b4[j]);
    }
    if ((lane & 24) == 0) {
      const int m = w * 64 + (lane & 7) * 8 + (lane >> 5) * 4;
      __builtin_amdgcn_raw_buffer_store_b128(enc(T, b4[0], b4[1], b4[2], b4[3]), rsw, (int)(WS_FIN + (((size_t)(m >> 4) * NWG + b) * 16 + (m & 15)) * 4), 0, 16);
    }
  }
  STAMPW(1, 7, W0);
  if (b < NFC) {
    v4u fv[2];
    unsigned spins = 0;
    int zoff = 0;
    for (;;) {
      asm volatile("" : "+v"(zoff));
      unsigned bad = 0u;
#pragma unroll
      for (int i = 0; i < 2; ++i) {
        fv[i] = __builtin_amdgcn_raw_buffer_load_b128(rsw, zoff + (int)(WS_FIN + ((size_t)b * NWG * 16 + (i * NT + t) * 4) * 4), 0, 16);
        bad |= dec(T, fv[i]);
      }
      if (FRESH(bad)) break;
      if (++spins > SPIN_CAP) break;
      __builtin_amdgcn_s_sleep(SLP);
    }
    STAMPW(1, 8, W0);
    f32x4 v;
    v[0] = xrow_sum(row_sum_s4(asf(fv[0].x) + asf(fv[1].x)));
    v[1] = xrow_sum(row_sum_s4(asf(fv[0].y) + asf(fv[1].y)));
    v[2] = xrow_sum(row_sum_s4(asf(fv[0].z) + asf(fv[1].z)));
    v[3] = xrow_sum(row_sum_s4(asf(fv[0].w) + asf(fv[1].w)));
    if (lane < 4) *(f32x4*)&small[w * 16 + lane * 4] = v;
    WG_BARRIER();
    if (t < 8) {
      float o0 = 0.f, o1 = 0.f;
#pragma unroll
      for (int i = 0; i < 8; ++i) { o0 += small[i * 16 + 2 * t]; o1 += small[i * 16 + 2 * t + 1]; }
      *(unsigned*)(out + b * 16 + 2 * t) = pack2(o0, o1);
    }
    STAMPW(1, 9, W0);
  }
}

extern "C" void kernel_launch(void* const* d_in, const int* in_sizes, int n_in, void* d_out, int out_size,
                              void* d_ws, size_t ws_size, hipStream_t stream) {
  const u16* x = (const u16*)d_in[0];
  const u16* c_kv = (const u16*)d_in[1];
  const u16* w_q = (const u16*)d_in[2];
  const u16* w_uk = (const u16*)d_in[3];
  const u16* w_uv = (const u16*)d_in[4];
  const u16* w_o = (const u16*)d_in[5];
  const u16* w_gate = (const u16*)d_in[7];
  const u16* w_up = (const u16*)d_in[8];
  const u16* w_down = (const u16*)d_in[9];
  const u16* gate_w = (const u16*)d_in[10];
  unsigned char* ws = (unsigned char*)d_ws;
  k2_attn<<<NWG, NTH, 0, stream>>>(x, c_kv, w_q, w_gate, w_up, w_uk, w_uv, w_o, w_down, gate_w, ws, (u16*)d_out);
}
```

```cpp
#include <hip/hip_runtime.h>
#include <hip/hip_bf16.h>

#define PD 1
#define PD1 1
#define LATE2 4
#define PRE_C 16
#define PRE_Q 0
#define PRE_A 24
#define B1 8
#define PRE_B 4
#define UVLATE
#define PFM 2
typedef unsigned short u16;
typedef unsigned v4u __attribute__((ext_vector_type(4)));
typedef unsigned v2u __attribute__((ext_vector_type(2)));
typedef float f32x4 __attribute__((ext_vector_type(4)));
typedef short bf16x8 __attribute__((ext_vector_type(8)));
typedef unsigned short u16x4 __attribute__((ext_vector_type(4)));

constexpr int H = 32, D = 128, MD = 512, DC = 512, SK = 8192, FF = 1376;
constexpr int NWG = 256, NT = 512;
constexpr int HG = 4;
constexpr int HPG = H / HG;
constexpr int NR = NWG / HG;
constexpr int KPR = SK / NR;
constexpr int NFC = 32;
constexpr int NFG = 43;
static_assert(HPG == 8 && KPR == 128, "geometry");

constexpr size_t WS_PART = 0;
constexpr size_t PART_BYTES = (size_t)H * 8 * NR * 64 * 4;
constexpr size_t WS_ML = WS_PART + PART_BYTES;
constexpr size_t ML_BYTES = (size_t)H * NR * 16;
constexpr size_t WS_FIN = WS_ML + ML_BYTES;
constexpr size_t FIN_BYTES = (size_t)NFC * NWG * 16 * 4;
constexpr size_t WS_CTX = WS_FIN + FIN_BYTES;
constexpr size_t CTX_BYTES = (size_t)H * 8 * D * 4;
constexpr size_t WS_FP = WS_CTX + CTX_BYTES;
constexpr size_t FP_BYTES = (size_t)(FF / 2) * 16 * 16;
constexpr size_t WS_QP = WS_FP + FP_BYTES;
constexpr size_t QP_BYTES = (size_t)H * 8 * D * 4;
constexpr size_t WS_QA = WS_QP + QP_BYTES;
constexpr size_t QA_BYTES = (size_t)H * DC * 4;
constexpr size_t WS_END = WS_QA + QA_BYTES;
constexpr int NTH = NT + 256;

constexpr unsigned SPIN_CAP = 1u << 20;
#define ALLV(c) __all(c)
#define SLP 4
#define NOPF
#define W0 256
#define ZAUX 0
#define STAMP(k, i) do { } while (0)
#define STAMPW(k, i, tid) do { } while (0)

__device__ __forceinline__ float asf(unsigned u) { return __builtin_bit_cast(float, u); }
__device__ __forceinline__ unsigned asu(float f) { return __builtin_bit_cast(unsigned, f); }
__device__ __forceinline__ float bf_lo(unsigned u) { return __builtin_bit_cast(float, u << 16); }
__device__ __forceinline__ float bf_hi(unsigned u) { return __builtin_bit_cast(float, u & 0xffff0000u); }
__device__ __forceinline__ float bf2f(u16 v) { return __builtin_bit_cast(float, (unsigned)v << 16); }
__device__ __forceinline__ u16 f2bf(float f) {
  __bf16 t = (__bf16)f;
  return __builtin_bit_cast(u16, t);
}
__device__ __forceinline__ unsigned pack2(float a, float b) { return (unsigned)f2bf(a) | ((unsigned)f2bf(b) << 16); }
__device__ __forceinline__ unsigned pack2t(float a, float b) { return (__builtin_bit_cast(unsigned, a) >> 16) | 1u | ((unsigned)f2bf(b) << 16); }
__device__ __forceinline__ unsigned bsel(unsigned m, unsigned a, unsigned b) {
  unsigned r;
  asm("v_bfi_b32 %0, %1, %2, %3" : "=v"(r) : "v"(m), "v"(a), "v"(b));
  return r;
}
extern "C" __device__ unsigned long long llvm_dispatch_id() __asm("llvm.amdgcn.dispatch.id");
__device__ __forceinline__ unsigned r20(float f) { return (__builtin_bit_cast(unsigned, f) + 0x800u) & 0xfffff000u; }
struct Tag { unsigned t0, t1, t2, t3; };
__device__ __forceinline__ v4u enc(const Tag& T, float a, float b, float c, float d) {
  return v4u{r20(a) | T.t0, r20(b) | T.t1, r20(c) | T.t2, r20(d) | T.t3};
}
__device__ __forceinline__ unsigned dec(const Tag& T, v4u& v) {
  v.x ^= T.t0; v.y ^= T.t1; v.z ^= T.t2; v.w ^= T.t3;
  return (v.x | v.y) | (v.z | v.w);
}
#define FRESH(bad) __all(((bad) & 0xfffu) == 0u)

__device__ __forceinline__ void unpack8(const v4u& v, float* o) {
  o[0] = bf_lo(v.x); o[1] = bf_hi(v.x); o[2] = bf_lo(v.y); o[3] = bf_hi(v.y);
  o[4] = bf_lo(v.z); o[5] = bf_hi(v.z); o[6] = bf_lo(v.w); o[7] = bf_hi(v.w);
}

template <int CTRL> __device__ __forceinline__ float dppf(float v) {
  return __builtin_bit_cast(float, __builtin_amdgcn_update_dpp(0, __builtin_bit_cast(int, v), CTRL, 0xf, 0xf, false));
}
__device__ __forceinline__ float row_sum(float v) {
  v += dppf<0x128>(v); v += dppf<0x124>(v); v += dppf<0x122>(v); v += dppf<0x121>(v);
  return v;
}
__device__ __forceinline__ float row_max(float v) {
  v = fmaxf(v, dppf<0x128>(v)); v = fmaxf(v, dppf<0x124>(v)); v = fmaxf(v, dppf<0x122>(v)); v = fmaxf(v, dppf<0x121>(v));
  return v;
}
__device__ __forceinline__ float row_sum_s4(float v) {
  v += dppf<0x128>(v); v += dppf<0x124>(v);
  return v;
}
__device__ __forceinline__ float rl(float v, int l) {
  return __builtin_bit_cast(float, __builtin_amdgcn_readlane(__builtin_bit_cast(int, v), l));
}
__device__ __forceinline__ float wave_sum(float v) {
  v = row_sum(v);
  return (rl(v, 0) + rl(v, 16)) + (rl(v, 32) + rl(v, 48));
}
__device__ __forceinline__ float wave_max(float v) {
  v = row_max(v);
  return fmaxf(fmaxf(rl(v, 0), rl(v, 16)), fmaxf(rl(v, 32), rl(v, 48)));
}
__device__ __forceinline__ float xrow_sum(float v) {
  auto a = __builtin_amdgcn_permlane16_swap(asu(v), asu(v), false, false);
  v = asf(a[0]) + asf(a[1]);
  auto b = __builtin_amdgcn_permlane32_swap(asu(v), asu(v), false, false);
  return asf(b[0]) + asf(b[1]);
}
__device__ __forceinline__ float xrow_max(float v) {
  auto a = __builtin_amdgcn_permlane16_swap(asu(v), asu(v), false, false);
  v = fmaxf(asf(a[0]), asf(a[1]));
  auto b = __builtin_amdgcn_permlane32_swap(asu(v), asu(v), false, false);
  return fmaxf(asf(b[0]), asf(b[1]));
}
#define NTW 0
#define NTC 0
#define NTSTR ""
__device__ __forceinline__ v4u ldw(const void* p) {
  return *(const v4u*)p;
}
__device__ __forceinline__ void glds16(const void* gsrc, unsigned lds_dst) {
  unsigned keep;
  asm volatile("s_mov_b32 %0, m0\n\ts_mov_b32 m0, %2\n\ts_nop 0\n\tglobal_load_lds_dwordx4 %1, off" NTSTR "\n\ts_mov_b32 m0, %0"
               : "=&s"(keep) : "v"(gsrc), "s"(lds_dst) : "memory");
}

__device__ __forceinline__ unsigned lds_rd(unsigned addr) {
  unsigned v;
  asm volatile("ds_read_b32 %0, %1\n\ts_waitcnt lgkmcnt(0)" : "=v"(v) : "v"(addr) : "memory");
  return __builtin_amdgcn_readfirstlane(v);
}
__device__ __forceinline__ void lds_wr(unsigned addr, unsigned v) {
  asm volatile("ds_write_b32 %0, %1\n\ts_waitcnt lgkmcnt(0)" :: "v"(addr), "v"(v) : "memory");
}

#define WG_BARRIER() do { asm volatile("s_waitcnt lgkmcnt(0)" ::: "memory"); __builtin_amdgcn_s_barrier(); asm volatile("" ::: "memory"); } while (0)

__device__ __forceinline__ void glds4(const void* gsrc, unsigned lds_dst) {
  unsigned keep;
  asm volatile("s_mov_b32 %0, m0\n\ts_mov_b32 m0, %2\n\ts_nop 0\n\tglobal_load_lds_dword %1, off sc1\n\ts_mov_b32 m0, %0"
               : "=&s"(keep) : "v"(gsrc), "s"(lds_dst) : "memory");
}
#define SD 4
typedef unsigned long long u64;
__device__ __forceinline__ void issue(v4u (&r)[1], const unsigned (&o)[1], const void* ws, u64 m) {
  asm volatile("s_mov_b64 exec, %3\n\tglobal_load_dwordx4 %0, %1, %2 sc1\n\ts_mov_b64 exec, -1"
               : "+v"(r[0]) : "v"(o[0]), "s"(ws), "s"(m) : "memory");
}
__device__ __forceinline__ void issue(v4u (&r)[2], const unsigned (&o)[2], const void* ws, u64 m) {
  asm volatile("s_mov_b64 exec, %5\n\tglobal_load_dwordx4 %0, %2, %4 sc1\n\tglobal_load_dwordx4 %1, %3, %4 sc1\n\ts_mov_b64 exec, -1"
               : "+v"(r[0]), "+v"(r[1]) : "v"(o[0]), "v"(o[1]), "s"(ws), "s"(m) : "memory");
}
__device__ __forceinline__ void issue(v4u (&r)[4], const unsigned (&o)[4], const void* ws, u64 m) {
  asm volatile("s_mov_b64 exec, %9\n\tglobal_load_dwordx4 %0, %4, %8 sc1\n\tglobal_load_dwordx4 %1, %5, %8 sc1\n\t"
               "global_load_dwordx4 %2, %6, %8 sc1\n\tglobal_load_dwordx4 %3, %7, %8 sc1\n\ts_mov_b64 exec, -1"
               : "+v"(r[0]), "+v"(r[1]), "+v"(r[2]), "+v"(r[3]) : "v"(o[0]), "v"(o[1]), "v"(o[2]), "v"(o[3]), "s"(ws), "s"(m) : "memory");
}
template <int C> __device__ __forceinline__ void vwait(v4u (&r)[1]) { asm volatile("s_waitcnt vmcnt(%1)" : "+v"(r[0]) : "n"(C) : "memory"); }
template <int C> __device__ __forceinline__ void vwait(v4u (&r)[2]) { asm volatile("s_waitcnt vmcnt(%2)" : "+v"(r[0]), "+v"(r[1]) : "n"(C) : "memory"); }
template <int C> __device__ __forceinline__ void vwait(v4u (&r)[4]) { asm volatile("s_waitcnt vmcnt(%4)" : "+v"(r[0]), "+v"(r[1]), "+v"(r[2]), "+v"(r[3]) : "n"(C) : "memory"); }
__device__ __forceinline__ void keep(const v4u (&r)[1]) { asm volatile("" :: "v"(r[0])); }
__device__ __forceinline__ void keep(const v4u (&r)[2]) { asm volatile("" :: "v"(r[0]), "v"(r[1])); }
__device__ __forceinline__ void keep(const v4u (&r)[4]) { asm volatile("" :: "v"(r[0]), "v"(r[1]), "v"(r[2]), "v"(r[3])); }
template <int N> __device__ __forceinline__ void poll_start(v4u (&r)[N], const unsigned (&o)[N], const void* ws) {
#pragma unroll
  for (int i = 0; i < N; ++i) r[i] = v4u{0u, 0u, 0u, 0u};
  issue(r, o, ws, ~0ull);
}
template <int N, int DEPTH, bool W1 = true> __device__ __forceinline__ unsigned poll_finish(const Tag& T, v4u (&r)[N], const unsigned (&o)[N], const void* ws) {
#pragma unroll
  for (int k = 2; k < DEPTH; ++k) { issue(r, o, ws, ~0ull); __builtin_amdgcn_s_sleep(SD); }
  u64 m = ~0ull;
  unsigned spins = 0;
  for (;;) {
    if (DEPTH > 1) issue(r, o, ws, m);
    if (W1 || spins) vwait<N * (DEPTH - 1)>(r);
    unsigned bad = 0u;
#pragma unroll
    for (int i = 0; i < N; ++i) bad |= ((r[i].x ^ T.t0) | (r[i].y ^ T.t1)) | ((r[i].z ^ T.t2) | (r[i].w ^ T.t3));
    m = __builtin_amdgcn_ballot_w64((bad & 0xfffu) != 0u);
    if (m == 0ull || ++spins > SPIN_CAP) break;
    if (DEPTH == 1) { __builtin_amdgcn_s_sleep(SLP); issue(r, o, ws, m); }
  }
  return spins;
}
__device__ __forceinline__ f32x4 pay(const Tag& T, const v4u& v) { return f32x4{asf(v.x ^ T.t0), asf(v.y ^ T.t1), asf(v.z ^ T.t2), asf(v.w ^ T.t3)}; }
__device__ __forceinline__ v4u aload(const void* p) {
  v4u r;
  asm volatile("global_load_dwordx4 %0, %1, off" : "=v"(r) : "v"(p) : "memory");
  return r;
}
__device__ __forceinline__ float vmax(float a, float b) { float r; asm("v_max_f32 %0, %1, %2" : "=v"(r) : "v"(a), "v"(b)); return r; }
template <int ROR> __device__ __forceinline__ float dpp_max(float v) {
  float r;
  asm("s_nop 1\n\tv_max_f32_dpp %0, %1, %1 row_ror:%2 row_mask:0xf bank_mask:0xf" : "=v"(r) : "v"(v), "n"(ROR));
  return r;
}
__device__ __forceinline__ float xrow_vmax(float v) {
  auto a = __builtin_amdgcn_permlane16_swap(asu(v), asu(v), false, false);
  v = vmax(asf(a[0]), asf(a[1]));
  auto b = __builtin_amdgcn_permlane32_swap(asu(v), asu(v), false, false);
  return vmax(asf(b[0]), asf(b[1]));
}
typedef __bf16 bf16x2 __attribute__((ext_vector_type(2)));
__device__ __forceinline__ float dot2(unsigned a, unsigned b, float c) {
  return __builtin_amdgcn_fdot2_f32_bf16(__builtin_bit_cast(bf16x2, a), __builtin_bit_cast(bf16x2, b), c, false);
}
#define SPINS(slot, tid, v) do { (void)(v); } while (0)
__device__ __forceinline__ unsigned swz(unsigned row) { return ((row & 3) << 1) | ((row >> 3) & 1) | (((row >> 2) ^ (row >> 3)) & 1) << 3; }
__device__ __forceinline__ unsigned lds_off(unsigned row, unsigned ch) { return 1024u * row + 16u * (ch ^ swz(row)); }

constexpr int PSTR = 144;
constexpr int QSTR = 528;
constexpr int LSTR = 72;

__global__ __launch_bounds__(NTH, 3) void k2_attn(const u16* __restrict__ x, const u16* __restrict__ c_kv,
                                                 const u16* __restrict__ w_q, const u16* __restrict__ w_gate,
                                                 const u16* __restrict__ w_up,
                                                 const u16* __restrict__ w_uk, const u16* __restrict__ w_uv,
                                                 const u16* __restrict__ w_o, const u16* __restrict__ w_down,
                                                 const u16* __restrict__ gate_w, unsigned char* __restrict__ ws,
                                                 u16* __restrict__ out) {
  __shared__ __attribute__((aligned(1024))) unsigned char ctile[KPR * 1024];
  __shared__ __attribute__((aligned(16))) u16 qb[8192];
  __shared__ __attribute__((aligned(16))) u16 pl[HPG * PSTR];
  __shared__ __attribute__((aligned(16))) float qst[4 * 128];
  __shared__ __attribute__((aligned(16))) u16 lb[16 * LSTR];
  __shared__ __attribute__((aligned(16))) float lst[16];
  __shared__ __attribute__((aligned(16))) float wmax[HPG * 8];
  __shared__ __attribute__((aligned(16))) float wsum[HPG * 8];
  __shared__ __attribute__((aligned(16))) float small[8 * 16 + 8];
  __shared__ __attribute__((aligned(16))) unsigned dummy[8 * 64];

  const int t = (threadIdx.x + NT) % NTH, lane = t & 63, w = t >> 6;
  const int b = blockIdx.x;
  const int xcd = b & 7, idx = b >> 3;
  const int r = xcd * 8 + (idx >> 2), g = idx & 3;
  const int ch_ = xcd * 4 + (idx & 3), cs = idx >> 2;
  const int rg = t >> 4, cg = t & 15;

  Tag T;
  {
    const unsigned long long M48 = (1ull << 48) - 1;
    unsigned long long u = (llvm_dispatch_id() & 0xffffffull) | ((((unsigned long long)__builtin_amdgcn_dispatch_ptr() >> 12) & 0xffffffull) << 24);
    u = (u + 0x5851f42d4c95ull) & M48;
    u ^= u >> 23; u = (u * 0x2545f4914f6dull) & M48;
    u ^= u >> 25; u = (u * 0x9e3779b97f4bull) & M48;
    u ^= u >> 24;
    T.t0 = (unsigned)u & 0xfffu; T.t1 = (unsigned)(u >> 12) & 0xfffu; T.t2 = (unsigned)(u >> 24) & 0xfffu; T.t3 = (unsigned)(u >> 36) & 0xfffu;
  }
  asm volatile("" :: "s"(x), "s"(c_kv), "s"(w_q), "s"(w_gate), "s"(w_up), "s"(w_uk), "s"(w_uv), "s"(w_o), "s"(w_down), "s"(gate_w), "s"(ws), "s"(out),
                     "s"(T.t0), "s"(T.t1), "s"(T.t2), "s"(T.t3));
  const __amdgpu_buffer_rsrc_t rsw = __builtin_amdgcn_make_buffer_rsrc((void*)ws, 0, (int)WS_END, 0x00020000);

  v4u wuv[2];
  v4u afr[16];
  const bool has_ffn = b < FF / 8;
  const int fb = has_ffn ? b : 0;
  STAMPW(0, 0, 0); STAMPW(1, 0, W0);
  if (w < 8) {
    u16 x_raw, gw_raw;
    const unsigned cbase = __builtin_amdgcn_readfirstlane((unsigned)(size_t)ctile + w * 16384);
#pragma unroll
    for (int i = 0; i < 16; ++i) {
      if (i == B1) {
        x_raw = x[t]; gw_raw = gate_w[t];
        STAMPW(1, 18, W0); STAMPW(1, 19, W0 + 7 * 64);
        __builtin_amdgcn_s_barrier();
      }
      const unsigned row = w * 16 + i;
      glds16(c_kv + ((size_t)r * KPR + row) * DC + (lane ^ swz(i)) * 8, cbase + i * 1024);
    }
    STAMPW(1, 12, W0);
    unsigned xg = (unsigned)x_raw | ((unsigned)gw_raw << 16);
    asm volatile("s_waitcnt vmcnt(0)" : "+v"(xg) :: "memory");
    STAMPW(1, 11, W0); STAMPW(1, 16, W0 + 7 * 64); STAMPW(1, 17, W0 + 3 * 64);
    {
      const unsigned arow = (unsigned)(size_t)ctile + (w * 16 + (lane & 15)) * 1024u;
      const unsigned sw = swz(lane & 15), k4 = lane >> 4;
#pragma unroll
      for (int i = 0; i < 16; i += 4) {
        const unsigned a0 = arow + 16u * ((i * 4 + k4) ^ sw), a1 = arow + 16u * ((i * 4 + 4 + k4) ^ sw);
        const unsigned a2 = arow + 16u * ((i * 4 + 8 + k4) ^ sw), a3 = arow + 16u * ((i * 4 + 12 + k4) ^ sw);
        asm volatile("ds_read_b128 %0, %4\n\tds_read_b128 %1, %5\n\tds_read_b128 %2, %6\n\tds_read_b128 %3, %7"
                     : "=&v"(afr[i]), "=&v"(afr[i + 1]), "=&v"(afr[i + 2]), "=&v"(afr[i + 3]) : "v"(a0), "v"(a1), "v"(a2), "v"(a3) : "memory");
      }
    }
    {
      const float gp = wave_sum(bf_lo(xg) * bf_hi(xg));
      if (lane == 0) small[128 + w] = gp;
    }
  } else {
    const int hw = w - 8;
    const int hr = (t - NT) >> 4;
    v4u wq[4];
    u16 xr[4];
#pragma unroll
    for (int i = 0; i < 4; ++i) {
      const int row = cs * 64 + i * 16 + (lane >> 2);
      wq[i] = ldw(w_q + ((size_t)row * H + ch_) * D + hw * 32 + (lane & 3) * 8);
      xr[i] = x[row];
    }
    const bool ffn_p = b < 4 * NFG;
    const int fg = ffn_p ? b % NFG : 0, fk = (ffn_p ? b / NFG : 0) * 4 + hw;
    u16 xf[2];
    xf[0] = x[fk * 32 + (lane >> 2)]; xf[1] = x[fk * 32 + 16 + (lane >> 2)];
    v4u wk[4];
#pragma unroll
    for (int i = 0; i < 4; ++i) wk[i] = ldw(w_uk + ((size_t)(cs * 64 + 4 * hr + i) * H + ch_) * D + cg * 8);
    {
      const unsigned x01 = (unsigned)xr[0] | ((unsigned)xr[1] << 16), x23 = (unsigned)xr[2] | ((unsigned)xr[3] << 16);
      float acc[8];
#pragma unroll
      for (int j = 0; j < 4; ++j) {
        acc[2 * j] = dot2(__builtin_amdgcn_perm(wq[3][j], wq[2][j], 0x05040100u), x23, dot2(__builtin_amdgcn_perm(wq[1][j], wq[0][j], 0x05040100u), x01, 0.f));
        acc[2 * j + 1] = dot2(__builtin_amdgcn_perm(wq[3][j], wq[2][j], 0x07060302u), x23, dot2(__builtin_amdgcn_perm(wq[1][j], wq[0][j], 0x07060302u), x01, 0.f));
      }
      float b4[4];
#pragma unroll
      for (int j = 0; j < 4; ++j) {
        auto sw = __builtin_amdgcn_permlane32_swap(asu(acc[j]), asu(acc[j + 4]), false, false);
        b4[j] = row_sum_s4(asf(sw[0]) + asf(sw[1]));
        auto s2 = __builtin_amdgcn_permlane16_swap(asu(b4[j]), asu(b4[j]), false, false);
        b4[j] = asf(s2[0]) + asf(s2[1]);
      }
      if ((lane & 28) == 0) __builtin_amdgcn_raw_buffer_store_b128(enc(T, b4[0], b4[1], b4[2], b4[3]), rsw, (int)(WS_QP + ((ch_ * 8 + cs) * D + hw * 32 + (lane & 3) * 8 + (lane >> 5) * 4) * 4), 0, 16);
    }
    STAMPW(1, 20, 0);
    float wkf[32];
#pragma unroll
    for (int i = 0; i < 4; ++i) unpack8(wk[i], wkf + 8 * i);
#pragma unroll
    for (int i = 0; i < 32; i += 8)
      asm volatile("" : "+v"(wkf[i]), "+v"(wkf[i + 1]), "+v"(wkf[i + 2]), "+v"(wkf[i + 3]), "+v"(wkf[i + 4]), "+v"(wkf[i + 5]), "+v"(wkf[i + 6]), "+v"(wkf[i + 7]));
    asm volatile("" : "+v"(xf[0]), "+v"(xf[1]));
    __builtin_amdgcn_s_sleep(PRE_Q);
    v4u qg[4];
    {
      unsigned o[4];
#pragma unroll
      for (int i = 0; i < 4; ++i) o[i] = (unsigned)(WS_QP + ((ch_ * 8 + (lane >> 5) + 2 * i) * D + (lane & 31) * 4) * 4);
      poll_start(qg, o, ws);
      if (PD > 1) __builtin_amdgcn_s_sleep(SD);
      const unsigned sp = poll_finish<4, PD>(T, qg, o, ws);
      STAMPW(1, 21, 0);
      SPINS(1, 0, sp);
    }
    float* qw = qst + hw * D;
    {
      f32x4 q4 = (pay(T, qg[0]) + pay(T, qg[1])) + (pay(T, qg[2]) + pay(T, qg[3]));
#pragma unroll
      for (int j = 0; j < 4; ++j) {
        auto sw = __builtin_amdgcn_permlane32_swap(asu(q4[j]), asu(q4[j]), false, false);
        q4[j] = asf(sw[0]) + asf(sw[1]);
      }
      if (lane < 32) *(f32x4*)&qw[lane * 4] = q4;
    }
    asm volatile("s_waitcnt lgkmcnt(0)" ::: "memory");
    const float scale = 0.08838834764831845f * 1.4426950408889634f;
    const f32x4 q0 = *(const f32x4*)&qw[cg * 8], q1 = *(const f32x4*)&qw[cg * 8 + 4];
    float v[4];
#pragma unroll
    for (int i = 0; i < 4; ++i) {
      const float* f = wkf + 8 * i;
      float u = (q0[0] * f[0] + q0[1] * f[1]) + (q0[2] * f[2] + q0[3] * f[3]) + (q1[0] * f[4] + q1[1] * f[5]) + (q1[2] * f[6] + q1[3] * f[7]);
      v[i] = row_sum(u) * scale;
    }
    if (cg == 0) __builtin_amdgcn_raw_buffer_store_b128(enc(T, v[0], v[1], v[2], v[3]), rsw, (int)(WS_QA + (ch_ * DC + cs * 64 + 4 * hr) * 4), 0, 16);
    __builtin_amdgcn_s_barrier();
    STAMPW(1, 1, 0);
    v4u wg[2], wu[2];
#pragma unroll
    for (int i = 0; i < 2; ++i) {
      const int row = fk * 32 + i * 16 + (lane >> 2);
      wg[i] = aload(w_gate + (size_t)row * FF + fg * 32 + (lane & 3) * 8);
      wu[i] = aload(w_up + (size_t)row * FF + fg * 32 + (lane & 3) * 8);
    }
    __builtin_amdgcn_s_sleep(PRE_A);
    v4u ag[4];
    {
      unsigned o[4];
#pragma unroll
      for (int i = 0; i < 4; ++i) o[i] = (unsigned)(WS_QA + g * HPG * DC * 4 + (i * 256 + t - NT) * 16);
      poll_start(ag, o, ws);
      asm volatile("s_waitcnt vmcnt(4)" : "+v"(wg[0]), "+v"(wg[1]), "+v"(wu[0]), "+v"(wu[1]) :: "memory");
      keep(qg);
      {
        const unsigned x01 = (unsigned)xf[0] | ((unsigned)xf[1] << 16);
        float acc[16];
#pragma unroll
        for (int j = 0; j < 4; ++j) {
          acc[4 * j] = dot2(__builtin_amdgcn_perm(wg[1][j], wg[0][j], 0x05040100u), x01, 0.f);
          acc[4 * j + 1] = dot2(__builtin_amdgcn_perm(wu[1][j], wu[0][j], 0x05040100u), x01, 0.f);
          acc[4 * j + 2] = dot2(__builtin_amdgcn_perm(wg[1][j], wg[0][j], 0x07060302u), x01, 0.f);
          acc[4 * j + 3] = dot2(__builtin_amdgcn_perm(wu[1][j], wu[0][j], 0x07060302u), x01, 0.f);
        }
        float h8[8], h4[4];
#pragma unroll
        for (int j = 0; j < 8; ++j) {
          auto sw = __builtin_amdgcn_permlane32_swap(asu(acc[j]), asu(acc[j + 8]), false, false);
          h8[j] = asf(sw[0]) + asf(sw[1]);
        }
#pragma unroll
        for (int j = 0; j < 4; ++j) {
          auto sw = __builtin_amdgcn_permlane16_swap(asu(h8[j]), asu(h8[j + 4]), false, false);
          h4[j] = row_sum_s4(asf(sw[0]) + asf(sw[1]));
        }
        if (ffn_p) {
          if ((lane & 12) == 0)
            __builtin_amdgcn_raw_buffer_store_b128(enc(T, h4[0], h4[1], h4[2], h4[3]), rsw,
                                                   (int)(WS_FP + ((fg * 16 + (lane & 3) * 4 + (lane >> 4)) * 16 + fk) * 16), 0, 16);
          vwait<1>(ag);
        } else {
          vwait<0>(ag);
        }
      }
      STAMPW(1, 24, 0);
      const unsigned sp = poll_finish<4, 1, false>(T, ag, o, ws);
      STAMPW(1, 10, 0); STAMPW(1, 13, 64); STAMPW(1, 14, 128); STAMPW(1, 15, 192);
      SPINS(2, 0, sp);
      {
        const int hl = t - NT;
        size_t po;
        if (hl < 128) po = WS_PART + ((size_t)ch_ * 8 + cs) * 16384 + hl * 128;
        else if (hl < 144) po = WS_FIN + (size_t)(b & (NFC - 1)) * 16384 + ((b >> 5) * 16 + (hl & 15)) * 128;
        else if (hl < 148) po = WS_CTX + (size_t)ch_ * 4096 + (cs * 4 + (hl & 3)) * 128;
        else po = WS_ML + (size_t)ch_ * 1024 + cs * 128;
        if (hl < 149) glds4(ws + po, __builtin_amdgcn_readfirstlane((unsigned)(size_t)dummy + hw * 256));
      }
    }
#pragma unroll
    for (int i = 0; i < 4; ++i) {
      const f32x4 a = pay(T, ag[i]);
      *(v2u*)(qb + (i * 2 + (hw >> 1)) * QSTR + ((hw & 1) * 64 + lane) * 4) = v2u{pack2(a[0], a[1]), pack2(a[2], a[3])};
    }
    WG_BARRIER();
    return;
  }
  WG_BARRIER();
  STAMPW(1, 2, W0);

  f32x4 sacc;
  u16x4 vt[32];
  {
    v4u bfr[16];
    const unsigned bad_ = (unsigned)(size_t)qb + (lane & 7) * (QSTR * 2) + (lane >> 4) * 16;
    asm volatile("ds_read_b128 %0, %16\n\tds_read_b128 %1, %16 offset:64\n\tds_read_b128 %2, %16 offset:128\n\tds_read_b128 %3, %16 offset:192\n\t"
                 "ds_read_b128 %4, %16 offset:256\n\tds_read_b128 %5, %16 offset:320\n\tds_read_b128 %6, %16 offset:384\n\tds_read_b128 %7, %16 offset:448\n\t"
                 "ds_read_b128 %8, %16 offset:512\n\tds_read_b128 %9, %16 offset:576\n\tds_read_b128 %10, %16 offset:640\n\tds_read_b128 %11, %16 offset:704\n\t"
                 "ds_read_b128 %12, %16 offset:768\n\tds_read_b128 %13, %16 offset:832\n\tds_read_b128 %14, %16 offset:896\n\tds_read_b128 %15, %16 offset:960\n\t"
                 "s_waitcnt lgkmcnt(8)"
                 : "=&v"(bfr[0]), "=&v"(bfr[1]), "=&v"(bfr[2]), "=&v"(bfr[3]), "=&v"(bfr[4]), "=&v"(bfr[5]), "=&v"(bfr[6]), "=&v"(bfr[7]),
                   "=&v"(bfr[8]), "=&v"(bfr[9]), "=&v"(bfr[10]), "=&v"(bfr[11]), "=&v"(bfr[12]), "=&v"(bfr[13]), "=&v"(bfr[14]), "=&v"(bfr[15])
                 : "v"(bad_) : "memory");
    f32x4 s0 = {0.f, 0.f, 0.f, 0.f}, s1 = {0.f, 0.f, 0.f, 0.f};
#pragma unroll
    for (int i = 0; i < 8; i += 2) {
      s0 = __builtin_amdgcn_mfma_f32_16x16x32_bf16(__builtin_bit_cast(bf16x8, afr[i]), __builtin_bit_cast(bf16x8, bfr[i]), s0, 0, 0, 0);
      s1 = __builtin_amdgcn_mfma_f32_16x16x32_bf16(__builtin_bit_cast(bf16x8, afr[i + 1]), __builtin_bit_cast(bf16x8, bfr[i + 1]), s1, 0, 0, 0);
    }
    asm volatile("s_waitcnt lgkmcnt(0)" : "+v"(bfr[8]), "+v"(bfr[9]), "+v"(bfr[10]), "+v"(bfr[11]), "+v"(bfr[12]), "+v"(bfr[13]), "+v"(bfr[14]), "+v"(bfr[15]), "+v"(s0), "+v"(s1) :: "memory");
#pragma unroll
    for (int i = 8; i < 16; i += 2) {
      s0 = __builtin_amdgcn_mfma_f32_16x16x32_bf16(__builtin_bit_cast(bf16x8, afr[i]), __builtin_bit_cast(bf16x8, bfr[i]), s0, 0, 0, 0);
      s1 = __builtin_amdgcn_mfma_f32_16x16x32_bf16(__builtin_bit_cast(bf16x8, afr[i + 1]), __builtin_bit_cast(bf16x8, bfr[i + 1]), s1, 0, 0, 0);
    }
    sacc = s0 + s1;
  }
  auto trread = [&](int nt) {
    const unsigned cbase = (unsigned)(size_t)ctile;
    const unsigned gq = lane >> 4, q = (lane & 15) >> 2, p = lane & 3;
    const unsigned cc = w * 4 + nt;
    const unsigned ad0 = cbase + lds_off(8 * gq + q, 2 * cc + (p >> 1)) + 8 * (p & 1);
    const unsigned ad1 = cbase + lds_off(8 * gq + 4 + q, 2 * cc + (p >> 1)) + 8 * (p & 1);
    const unsigned ad2 = ad0 + 65536u, ad3 = ad1 + 65536u;
    asm volatile("ds_read_b64_tr_b16 %0, %8\n\tds_read_b64_tr_b16 %1, %9\n\tds_read_b64_tr_b16 %2, %8 offset:32768\n\tds_read_b64_tr_b16 %3, %9 offset:32768\n\t"
                 "ds_read_b64_tr_b16 %4, %10\n\tds_read_b64_tr_b16 %5, %11\n\tds_read_b64_tr_b16 %6, %10 offset:32768\n\tds_read_b64_tr_b16 %7, %11 offset:32768"
                 : "=&v"(vt[nt * 8]), "=&v"(vt[nt * 8 + 1]), "=&v"(vt[nt * 8 + 2]), "=&v"(vt[nt * 8 + 3]), "=&v"(vt[nt * 8 + 4]), "=&v"(vt[nt * 8 + 5]), "=&v"(vt[nt * 8 + 6]), "=&v"(vt[nt * 8 + 7])
                 : "v"(ad0), "v"(ad1), "v"(ad2), "v"(ad3) : "memory");
  };
  trread(0); trread(1);
  {
    const float mx = xrow_vmax(vmax(vmax(sacc[0], sacc[1]), vmax(sacc[2], sacc[3])));
    if (lane < 8) wmax[lane * 8 + w] = mx;
  }
  STAMPW(1, 22, W0);
  WG_BARRIER();
  STAMPW(1, 23, W0);
  float mref;
  {
    f32x4 m0, m1;
    asm volatile("ds_read_b128 %0, %2\n\tds_read_b128 %1, %2 offset:16" : "=&v"(m0), "=&v"(m1) : "v"((unsigned)(size_t)wmax + (lane & 7) * 32) : "memory");
    trread(2);
    asm volatile("s_waitcnt lgkmcnt(8)" : "+v"(m0), "+v"(m1) :: "memory");
    const float mx = vmax(vmax(vmax(m0[0], m0[1]), vmax(m0[2], m0[3])), vmax(vmax(m1[0], m1[1]), vmax(m1[2], m1[3])));
    mref = asf(r20(mx));
    const unsigned p01 = pack2(__builtin_amdgcn_exp2f(sacc[0] - mref), __builtin_amdgcn_exp2f(sacc[1] - mref));
    const unsigned p23 = pack2(__builtin_amdgcn_exp2f(sacc[2] - mref), __builtin_amdgcn_exp2f(sacc[3] - mref));
    const float l = xrow_sum((bf_lo(p01) + bf_hi(p01)) + (bf_lo(p23) + bf_hi(p23)));
    if ((lane & 15) < 8) {
      *(v2u*)(pl + (lane & 7) * PSTR + w * 16 + (lane >> 4) * 4) = v2u{p01, p23};
      if (lane < 8) wsum[lane * 8 + w] = l;
    }
  }
  trread(3);
  STAMPW(1, 25, W0);
  asm volatile("s_waitcnt lgkmcnt(8)" ::: "memory");
  __builtin_amdgcn_s_barrier();
  asm volatile("" ::: "memory");
  STAMPW(1, 26, W0);
  {
    v4u pf[4];
#pragma unroll
    for (int ks = 0; ks < 4; ++ks) pf[ks] = *(const v4u*)(pl + (lane & 7) * PSTR + ks * 32 + (lane >> 4) * 8);
    if (w == 0 && lane < 8) {
      const f32x4 s0 = *(const f32x4*)&wsum[lane * 8], s1 = *(const f32x4*)&wsum[lane * 8 + 4];
      const float l = ((s0[0] + s0[1]) + (s0[2] + s0[3])) + ((s1[0] + s1[1]) + (s1[2] + s1[3]));
      const float lh = asf(r20(l));
      __builtin_amdgcn_raw_buffer_store_b128(enc(T, mref, lh, l - lh, 0.f), rsw, (int)(WS_ML + ((size_t)(g * HPG + lane) * NR + r) * 16), 0, 16);
    }
    asm volatile("s_waitcnt lgkmcnt(0)" : "+v"(pf[0]), "+v"(pf[1]), "+v"(pf[2]), "+v"(pf[3]) :: "memory");
    f32x4 acc[4];
#pragma unroll
    for (int nt = 0; nt < 4; ++nt) acc[nt] = f32x4{0.f, 0.f, 0.f, 0.f};
#pragma unroll
    for (int ks = 0; ks < 4; ++ks) {
#pragma unroll
      for (int nt = 0; nt < 4; ++nt) {
        const u16x4 a0 = vt[nt * 8 + 2 * ks], a1 = vt[nt * 8 + 2 * ks + 1];
        bf16x8 af;
        af[0] = a0[0]; af[1] = a0[1]; af[2] = a0[2]; af[3] = a0[3];
        af[4] = a1[0]; af[5] = a1[1]; af[6] = a1[2]; af[7] = a1[3];
        acc[nt] = __builtin_amdgcn_mfma_f32_16x16x32_bf16(af, __builtin_bit_cast(bf16x8, pf[ks]), acc[nt], 0, 0, 0);
      }
    }
    if ((lane & 15) < 8) {
#pragma unroll
      for (int nt = 0; nt < 4; ++nt) {
        const size_t off = WS_PART + ((((size_t)(g * HPG + (lane & 15)) * 8 + w) * NR + r) * 64 + nt * 16 + (lane >> 4) * 4) * 4;
        __builtin_amdgcn_raw_buffer_store_b128(enc(T, acc[nt][0], acc[nt][1], acc[nt][2], acc[nt][3]), rsw, (int)off, 0, 16);
      }
    }
  }
  STAMPW(1, 3, W0);
#pragma unroll
  for (int k = 0; k < 2; ++k) wuv[k] = aload(w_uv + ((size_t)(cs * 64 + (lane >> 1) + 32 * k) * H + ch_) * D + w * 16 + (lane & 1) * 8);
  v4u wo0 = aload(w_o + ((size_t)ch_ * D + cs * 16 + (lane >> 3) * 2) * MD + w * 64 + (lane & 7) * 8);
  v4u wo1 = aload(w_o + ((size_t)ch_ * D + cs * 16 + (lane >> 3) * 2 + 1) * MD + w * 64 + (lane & 7) * 8);
  v4u wd = aload(w_down + ((size_t)fb * 8 + (lane >> 3)) * MD + w * 64 + (lane & 7) * 8);
  v4u fpv[1];
  const unsigned fpo[1] = {(unsigned)(WS_FP + (size_t)fb * 1024 + lane * 16)};
  poll_start(fpv, fpo, ws);

  v4u pb[4];
  {
    unsigned o[4];
#pragma unroll
    for (int i = 0; i < 2; ++i) {
      o[i] = (unsigned)(WS_ML + ((size_t)ch_ * NR + rg + 32 * i) * 16);
      o[2 + i] = (unsigned)(WS_PART + ((((size_t)ch_ * 8 + cs) * NR + rg + 32 * i) * 64 + cg * 4) * 4);
    }
    asm volatile("s_waitcnt vmcnt(4)" : "+v"(wuv[0]), "+v"(wuv[1]) :: "memory");
    u16x4 ut[4];
    {
      const unsigned sb = (unsigned)(size_t)qb + w * 2048;
      const unsigned wa = sb + lane * 16;
      const unsigned ra = sb + ((lane >> 4) * 8 + ((lane & 15) >> 2)) * 32 + (lane & 3) * 8;
      asm volatile("ds_write_b128 %4, %6\n\tds_write_b128 %4, %7 offset:1024\n\ts_waitcnt lgkmcnt(0)\n\t"
                   "ds_read_b64_tr_b16 %0, %5\n\tds_read_b64_tr_b16 %1, %5 offset:128\n\tds_read_b64_tr_b16 %2, %5 offset:1024\n\tds_read_b64_tr_b16 %3, %5 offset:1152\n\ts_waitcnt lgkmcnt(0)"
                   : "=&v"(ut[0]), "=&v"(ut[1]), "=&v"(ut[2]), "=&v"(ut[3]) : "v"(wa), "v"(ra), "v"(wuv[0]), "v"(wuv[1]) : "memory");
    }
    __builtin_amdgcn_s_sleep(PRE_B);
    poll_start(pb, o, ws);
    const unsigned sp = poll_finish<4, PD>(T, pb, o, ws);
    asm volatile("" : "+v"(wo0), "+v"(wo1), "+v"(wd), "+v"(fpv[0]));
    STAMPW(1, 4, W0);
    SPINS(3, W0, sp);
    const f32x4 ma = pay(T, pb[0]), mb = pay(T, pb[1]), pa = pay(T, pb[2]), pc = pay(T, pb[3]);
    const float Mw = xrow_vmax(vmax(ma[0], mb[0]));
    const float ea = __builtin_amdgcn_exp2f(ma[0] - Mw), eb = __builtin_amdgcn_exp2f(mb[0] - Mw);
    const f32x4 a4 = ea * pa + eb * pc;
    const float lw = xrow_sum(ea * (ma[1] + ma[2]) + eb * (mb[1] + mb[2]));
    auto s0 = __builtin_amdgcn_permlane32_swap(asu(a4[0]), asu(a4[2]), false, false);
    auto s1 = __builtin_amdgcn_permlane32_swap(asu(a4[1]), asu(a4[3]), false, false);
    auto s2 = __builtin_amdgcn_permlane16_swap(asu(asf(s0[0]) + asf(s0[1])), asu(asf(s1[0]) + asf(s1[1])), false, false);
    const float lv = asf(s2[0]) + asf(s2[1]);
    const u16 hi = f2bf(lv), lo = f2bf(lv - bf2f(hi));
    lb[(2 * w) * LSTR + cg * 4 + (lane >> 4)] = hi;
    lb[(2 * w + 1) * LSTR + cg * 4 + (lane >> 4)] = lo;
    if (lane == 0) *(v2u*)&lst[2 * w] = v2u{asu(Mw), asu(lw)};
    STAMPW(1, 28, W0);
    WG_BARRIER();
    STAMPW(1, 29, W0);
    const v4u b0 = *(const v4u*)(lb + cg * LSTR + (lane >> 4) * 8), b1 = *(const v4u*)(lb + cg * LSTR + 32 + (lane >> 4) * 8);
    const v2u st = *(const v2u*)&lst[cg & 14];
    bf16x8 a0, a1;
    a0[0] = ut[0][0]; a0[1] = ut[0][1]; a0[2] = ut[0][2]; a0[3] = ut[0][3]; a0[4] = ut[1][0]; a0[5] = ut[1][1]; a0[6] = ut[1][2]; a0[7] = ut[1][3];
    a1[0] = ut[2][0]; a1[1] = ut[2][1]; a1[2] = ut[2][2]; a1[3] = ut[2][3]; a1[4] = ut[3][0]; a1[5] = ut[3][1]; a1[6] = ut[3][2]; a1[7] = ut[3][3];
    f32x4 dd = {0.f, 0.f, 0.f, 0.f};
    dd = __builtin_amdgcn_mfma_f32_16x16x32_bf16(a0, __builtin_bit_cast(bf16x8, b0), dd, 0, 0, 0);
    dd = __builtin_amdgcn_mfma_f32_16x16x32_bf16(a1, __builtin_bit_cast(bf16x8, b1), dd, 0, 0, 0);
    const float M = dpp_max<2>(dpp_max<4>(dpp_max<8>(asf(st.x))));
    const float f = __builtin_amdgcn_exp2f(asf(st.x) - M);
    float L = f * asf(st.y);
    L += dppf<0x128>(L); L += dppf<0x124>(L); L += dppf<0x122>(L);
    const float inv = __builtin_amdgcn_rcpf(L);
    const float e0 = row_sum(dd[0] * f) * inv, e1 = row_sum(dd[1] * f) * inv, e2 = row_sum(dd[2] * f) * inv, e3 = row_sum(dd[3] * f) * inv;
    if (cg == 0) __builtin_amdgcn_raw_buffer_store_b128(enc(T, e0, e1, e2, e3), rsw, (int)(WS_CTX + ((size_t)(ch_ * 8 + cs) * D + w * 16 + (lane >> 4) * 4) * 4), 0, 16);
  }
  STAMPW(1, 5, W0);
  v4u cv[1];
  {
    const unsigned o[1] = {(unsigned)(WS_CTX + ((size_t)(ch_ * 8 + (lane & 7)) * D + cs * 16 + (lane >> 4) * 4) * 4)};
    float gs = 0.f;
#pragma unroll
    for (int i = 0; i < 8; ++i) gs += small[128 + i];
    const float gatev = 1.f / (1.f + __expf(-gs));
    {
      unsigned spins = 0;
      v4u fv = fpv[0];
      unsigned bad = dec(T, fv);
      while (!FRESH(bad) && ++spins < SPIN_CAP) {
        __builtin_amdgcn_s_sleep(SLP);
        issue(fpv, fpo, ws, ~0ull);
        vwait<0>(fpv);
        fv = fpv[0];
        bad = dec(T, fv);
      }
      fpv[0] = fv;
    }
    const float g0 = row_sum(asf(fpv[0].x)), u0 = row_sum(asf(fpv[0].y)), g1 = row_sum(asf(fpv[0].z)), u1 = row_sum(asf(fpv[0].w));
    const float ga = (lane & 8) ? g1 : g0;
    const float ua = (lane & 8) ? u1 : u0;
    const float hv = ga / (1.f + __expf(-ga)) * ua;
    const float hh = has_ffn ? hv * (1.f - gatev) : 0.f;
    float a8[8], f0[8], f1[8];
    {
      float f[8];
      unpack8(wd, f);
      unpack8(wo0, f0);
      unpack8(wo1, f1);
#pragma unroll
      for (int j = 0; j < 8; ++j) { a8[j] = hh * f[j]; f0[j] *= gatev; f1[j] *= gatev; }
    }
    asm volatile("" : "+v"(a8[0]), "+v"(a8[1]), "+v"(a8[2]), "+v"(a8[3]), "+v"(a8[4]), "+v"(a8[5]), "+v"(a8[6]), "+v"(a8[7]));
    asm volatile("" : "+v"(f0[0]), "+v"(f0[1]), "+v"(f0[2]), "+v"(f0[3]), "+v"(f0[4]), "+v"(f0[5]), "+v"(f0[6]), "+v"(f0[7]));
    asm volatile("" : "+v"(f1[0]), "+v"(f1[1]), "+v"(f1[2]), "+v"(f1[3]), "+v"(f1[4]), "+v"(f1[5]), "+v"(f1[6]), "+v"(f1[7]));
    __builtin_amdgcn_s_sleep(LATE2);
    poll_start(cv, o, ws);
    const unsigned sp = poll_finish<1, PD1>(T, cv, o, ws);
    keep(pb);
    STAMPW(1, 6, W0);
    SPINS(4, W0, sp);
    const f32x4 cc = pay(T, cv[0]);
    float c0 = (lane & 8) ? cc[2] : cc[0], c1 = (lane & 8) ? cc[3] : cc[1];
    c0 += dppf<0xB1>(c0); c1 += dppf<0xB1>(c1);
    c0 += dppf<0x4E>(c0); c1 += dppf<0x4E>(c1);
    c0 += dppf<0x141>(c0); c1 += dppf<0x141>(c1);
#pragma unroll
    for (int j = 0; j < 8; ++j) a8[j] += c0 * f0[j] + c1 * f1[j];
    float b4[4];
#pragma unroll
    for (int j = 0; j < 4; ++j) {
      auto sw = __builtin_amdgcn_permlane32_swap(asu(a8[j]), asu(a8[j + 4]), false, false);
      b4[j] = asf(sw[0]) + asf(sw[1]);
      auto s2 = __builtin_amdgcn_permlane16_swap(asu(b4[j]), asu(b4[j]), false, false);
      b4[j] = asf(s2[0]) + asf(s2[1]);
      b4[j] += dppf<0x128>(b4[j]);
    }
    if ((lane & 24) == 0) {
      const int m = w * 64 + (lane & 7) * 8 + (lane >> 5) * 4;
      __builtin_amdgcn_raw_buffer_store_b128(enc(T, b4[0], b4[1], b4[2], b4[3]), rsw, (int)(WS_FIN + (((size_t)(m >> 4) * NWG + b) * 16 + (m & 15)) * 4), 0, 16);
    }
  }
  STAMPW(1, 7, W0);
  if (b < NFC) {
    v4u fv[2];
    unsigned o[2];
#pragma unroll
    for (int i = 0; i < 2; ++i) o[i] = (unsigned)(WS_FIN + ((size_t)b * NWG * 16 + (i * NT + t) * 4) * 4);
    __builtin_amdgcn_s_sleep(PRE_C);
    poll_start(fv, o, ws);
    if (PD1 > 1) __builtin_amdgcn_s_sleep(SD);
    const unsigned sp = poll_finish<2, PD1>(T, fv, o, ws);
    STAMPW(1, 8, W0);
    SPINS(5, W0, sp);
    const f32x4 s4 = pay(T, fv[0]) + pay(T, fv[1]);
    f32x4 v;
    v[0] = xrow_sum(row_sum_s4(s4[0]));
    v[1] = xrow_sum(row_sum_s4(s4[1]));
    v[2] = xrow_sum(row_sum_s4(s4[2]));
    v[3] = xrow_sum(row_sum_s4(s4[3]));
    if (lane < 4) *(f32x4*)&small[w * 16 + lane * 4] = v;
    WG_BARRIER();
    if (t < 8) {
      float o0 = 0.f, o1 = 0.f;
#pragma unroll
      for (int i = 0; i < 8; ++i) { o0 += small[i * 16 + 2 * t]; o1 += small[i * 16 + 2 * t + 1]; }
      *(unsigned*)(out + b * 16 + 2 * t) = pack2(o0, o1);
    }
    keep(fv);
    STAMPW(1, 9, W0);
  }
  keep(cv);
}

extern "C" void kernel_launch(void* const* d_in, const int* in_sizes, int n_in, void* d_out, int out_size,
                              void* d_ws, size_t ws_size, hipStream_t stream) {
  const u16* x = (const u16*)d_in[0];
  const u16* c_kv = (const u16*)d_in[1];
  const u16* w_q = (const u16*)d_in[2];
  const u16* w_uk = (const u16*)d_in[3];
  const u16* w_uv = (const u16*)d_in[4];
  const u16* w_o = (const u16*)d_in[5];
  const u16* w_gate = (const u16*)d_in[7];
  const u16* w_up = (const u16*)d_in[8];
  const u16* w_down = (const u16*)d_in[9];
  const u16* gate_w = (const u16*)d_in[10];
  unsigned char* ws = (unsigned char*)d_ws;
  k2_attn<<<NWG, NTH, 0, stream>>>(x, c_kv, w_q, w_gate, w_up, w_uk, w_uv, w_o, w_down, gate_w, ws, (u16*)d_out);
}
```

```cpp
#define AMPST 99
#include <hip/hip_runtime.h>
#include <hip/hip_bf16.h>

#define WDELAY 16
#define B1 8
#define UVD 0
#define PRE_Q 8
#define TQ 0
#define PAW 4
#define TA 400
#define TSD 30
#define DB 150
#define DC_ 350
#define PRE_F 16
#define SLP 2
#define SLPC 2
#define ROTW 1
#define TS 0
#define UVP 1
#define MAPQ 0
typedef unsigned short u16;
typedef unsigned v4u __attribute__((ext_vector_type(4)));
typedef unsigned v2u __attribute__((ext_vector_type(2)));
typedef float f32x4 __attribute__((ext_vector_type(4)));
typedef float f32x2 __attribute__((ext_vector_type(2)));
typedef short bf16x8 __attribute__((ext_vector_type(8)));
typedef unsigned short u16x4 __attribute__((ext_vector_type(4)));
typedef unsigned long long u64;

constexpr int H = 32, D = 128, MD = 512, DC = 512, SK = 8192, FF = 1376;
constexpr int NWG = 256, NT = 512;
constexpr int NR = 64, KPR = 128;
constexpr int NFS = FF / 16;

constexpr size_t WS_PART = 0;
constexpr size_t PART_BYTES = (size_t)H * 4 * NR * 128 * 4;
constexpr size_t WS_ML = WS_PART + PART_BYTES;
constexpr size_t ML_BYTES = (size_t)H * NR * 16;
constexpr size_t WS_ANN = WS_ML + ML_BYTES;
constexpr size_t ANN_BYTES = (size_t)2 * NR * 128;
constexpr size_t WS_FIN = WS_ANN + ANN_BYTES;
constexpr size_t FIN_BYTES = (size_t)256 * 512 * 4;
constexpr size_t WS_QA = WS_FIN + FIN_BYTES;
constexpr size_t QA_BYTES = (size_t)H * DC * 4;
constexpr size_t WS_QP = WS_QA + QA_BYTES;
constexpr size_t QP_BYTES = (size_t)H * 8 * D * 4;
constexpr size_t WS_FP = WS_QP + QP_BYTES;
constexpr size_t FP_BYTES = (size_t)16 * (FF / 2) * 16;
constexpr size_t WS_END = WS_FP + FP_BYTES;

constexpr unsigned SPIN_CAP = 1u << 20;
#define STAMPW(i, tid) do { if (AMPST == (i) && w == (tid) / 64) amp = now() - t0; } while (0)
#define SPINS(slot, tid, v) do { (void)(v); } while (0)
#define DW(n) do { } while (0)
#define STAMP(i) STAMPW(i, 0)

__device__ __forceinline__ float asf(unsigned u) { return __builtin_bit_cast(float, u); }
__device__ __forceinline__ unsigned asu(float f) { return __builtin_bit_cast(unsigned, f); }
__device__ __forceinline__ float bf_lo(unsigned u) { return __builtin_bit_cast(float, u << 16); }
__device__ __forceinline__ float bf_hi(unsigned u) { return __builtin_bit_cast(float, u & 0xffff0000u); }
__device__ __forceinline__ float bf2f(u16 v) { return __builtin_bit_cast(float, (unsigned)v << 16); }
__device__ __forceinline__ u16 f2bf(float f) {
  __bf16 t = (__bf16)f;
  return __builtin_bit_cast(u16, t);
}
__device__ __forceinline__ unsigned pack2(float a, float b) { unsigned r; asm("v_cvt_pk_bf16_f32 %0, %1, %2" : "=v"(r) : "v"(a), "v"(b)); return r; }
extern "C" __device__ unsigned long long llvm_dispatch_id() __asm("llvm.amdgcn.dispatch.id");
struct Tag { unsigned t0, t1, t2, t3; };
__device__ __forceinline__ unsigned encw(float f, unsigned t) { return ((asu(f) + (0x800u - t)) & 0xfffff000u) | t; }
__device__ __forceinline__ v4u enc(const Tag& T, float a, float b, float c, float d) {
  return v4u{encw(a, T.t0), encw(b, T.t1), encw(c, T.t2), encw(d, T.t3)};
}
__device__ __forceinline__ unsigned chk(const Tag& T, unsigned bad, const v4u& v) {
  bad = __builtin_amdgcn_bitop3_b32(bad, v.x, T.t0, 0xF6);
  bad = __builtin_amdgcn_bitop3_b32(bad, v.y, T.t1, 0xF6);
  bad = __builtin_amdgcn_bitop3_b32(bad, v.z, T.t2, 0xF6);
  return __builtin_amdgcn_bitop3_b32(bad, v.w, T.t3, 0xF6);
}
__device__ __forceinline__ u64 stale(unsigned bad) { return __builtin_amdgcn_ballot_w64((bad & 0xfffu) != 0u); }
__device__ __forceinline__ f32x4 pay(const v4u& v) { return __builtin_bit_cast(f32x4, v); }

__device__ __forceinline__ void unpack8(const v4u& v, float* o) {
  o[0] = bf_lo(v.x); o[1] = bf_hi(v.x); o[2] = bf_lo(v.y); o[3] = bf_hi(v.y);
  o[4] = bf_lo(v.z); o[5] = bf_hi(v.z); o[6] = bf_lo(v.w); o[7] = bf_hi(v.w);
}
template <int CTRL> __device__ __forceinline__ float dppf(float v) {
  return __builtin_bit_cast(float, __builtin_amdgcn_update_dpp(0, __builtin_bit_cast(int, v), CTRL, 0xf, 0xf, false));
}
template <int CTRL> __device__ __forceinline__ unsigned dppu(unsigned v) {
  return (unsigned)__builtin_amdgcn_update_dpp(0, (int)v, CTRL, 0xf, 0xf, false);
}
__device__ __forceinline__ float row_sum(float v) {
  v += dppf<0x128>(v); v += dppf<0x124>(v); v += dppf<0x122>(v); v += dppf<0x121>(v);
  return v;
}
__device__ __forceinline__ float sw32(float a, float b) {
  auto s = __builtin_amdgcn_permlane32_swap(asu(a), asu(b), false, false);
  return asf(s[0]) + asf(s[1]);
}
__device__ __forceinline__ float sw16(float a, float b) {
  auto s = __builtin_amdgcn_permlane16_swap(asu(a), asu(b), false, false);
  return asf(s[0]) + asf(s[1]);
}
__device__ __forceinline__ float xrow_sum(float v) { v = sw16(v, v); return sw32(v, v); }
__device__ __forceinline__ float vmax(float a, float b) { float r; asm("v_max_f32 %0, %1, %2" : "=v"(r) : "v"(a), "v"(b)); return r; }
__device__ __forceinline__ float xrow_vmax(float v) {
  auto a = __builtin_amdgcn_permlane16_swap(asu(v), asu(v), false, false);
  v = vmax(asf(a[0]), asf(a[1]));
  auto b = __builtin_amdgcn_permlane32_swap(asu(v), asu(v), false, false);
  return vmax(asf(b[0]), asf(b[1]));
}
__device__ __forceinline__ void glds16(const void* gsrc, unsigned lds_dst) {
  unsigned keep;
  asm volatile("s_mov_b32 %0, m0\n\ts_mov_b32 m0, %2\n\ts_nop 0\n\tglobal_load_lds_dwordx4 %1, off\n\ts_mov_b32 m0, %0"
               : "=&s"(keep) : "v"(gsrc), "s"(lds_dst) : "memory");
}
__device__ __forceinline__ void glds16x4(const void* base, unsigned o0, unsigned o1, unsigned o2, unsigned o3, unsigned lds_dst) {
  unsigned keep;
  asm volatile("s_mov_b32 %0, m0\n\ts_mov_b32 m0, %6\n\ts_nop 0\n\tglobal_load_lds_dwordx4 %1, %5\n\tglobal_load_lds_dwordx4 %2, %5 offset:1024\n\t"
               "global_load_lds_dwordx4 %3, %5 offset:2048\n\tglobal_load_lds_dwordx4 %4, %5 offset:3072\n\ts_mov_b32 m0, %0"
               : "=&s"(keep) : "v"(o0), "v"(o1), "v"(o2), "v"(o3), "s"(base), "s"(lds_dst) : "memory");
}
#define WG_BARRIER() do { asm volatile("s_waitcnt lgkmcnt(0)" ::: "memory"); __builtin_amdgcn_s_barrier(); asm volatile("" ::: "memory"); } while (0)
#define VMWAIT(n) asm volatile("s_waitcnt vmcnt(%0)" :: "n"(n) : "memory")

__device__ __forceinline__ void poll1(v4u& r, unsigned o, const void* ws, u64 m) {
  asm volatile("s_mov_b64 exec, %3\n\tglobal_load_dwordx4 %0, %1, %2 sc1\n\ts_mov_b64 exec, -1\n\ts_waitcnt vmcnt(0)"
               : "+v"(r) : "v"(o), "s"(ws), "s"(m) : "memory");
}
__device__ __forceinline__ void poll2(v4u& r0, unsigned o0, u64 m0, v4u& r1, unsigned o1, u64 m1, const void* ws) {
  asm volatile("s_mov_b64 exec, %5\n\tglobal_load_dwordx4 %0, %2, %4 sc1\n\ts_mov_b64 exec, %6\n\tglobal_load_dwordx4 %1, %3, %4 sc1\n\t"
               "s_mov_b64 exec, -1\n\ts_waitcnt vmcnt(0)"
               : "+v"(r0), "+v"(r1) : "v"(o0), "v"(o1), "s"(ws), "s"(m0), "s"(m1) : "memory");
}
#define LD4 "global_load_dwordx4 %0, %4, %5 sc1\n\tglobal_load_dwordx4 %1, %4, %5 offset:1024 sc1\n\t" \
            "global_load_dwordx4 %2, %4, %5 offset:2048 sc1\n\tglobal_load_dwordx4 %3, %4, %5 offset:3072 sc1"
__device__ __forceinline__ void issue4(v4u* r, unsigned o, const void* ws) {
  asm volatile(LD4 : "=&v"(r[0]), "=&v"(r[1]), "=&v"(r[2]), "=&v"(r[3]) : "v"(o), "s"(ws) : "memory");
}
__device__ __forceinline__ void poll4(v4u* r, unsigned o, const void* ws, u64 m) {
  asm volatile("s_mov_b64 exec, %6\n\t" LD4 "\n\ts_mov_b64 exec, -1\n\ts_waitcnt vmcnt(0)"
               : "+v"(r[0]), "+v"(r[1]), "+v"(r[2]), "+v"(r[3]) : "v"(o), "s"(ws), "s"(m) : "memory");
}
__device__ __forceinline__ void poll5(v4u* r, unsigned o, v4u& e, unsigned oe, const void* ws, u64 m) {
  asm volatile("s_mov_b64 exec, %7\n\tglobal_load_dwordx4 %4, %8, %6 sc1\n\tglobal_load_dwordx4 %0, %5, %6 sc1\n\tglobal_load_dwordx4 %1, %5, %6 offset:1024 sc1\n\t"
               "global_load_dwordx4 %2, %5, %6 offset:2048 sc1\n\tglobal_load_dwordx4 %3, %5, %6 offset:3072 sc1\n\ts_mov_b64 exec, -1\n\ts_waitcnt vmcnt(0)"
               : "+v"(r[0]), "+v"(r[1]), "+v"(r[2]), "+v"(r[3]), "+v"(e) : "v"(o), "s"(ws), "s"(m), "v"(oe) : "memory");
}
template <int C> __device__ __forceinline__ void vw4(v4u* r) { asm volatile("s_waitcnt vmcnt(%4)" : "+v"(r[0]), "+v"(r[1]), "+v"(r[2]), "+v"(r[3]) : "n"(C) : "memory"); }
__device__ __forceinline__ v4u aload(const void* base, unsigned o) {
  v4u r;
  asm volatile("global_load_dwordx4 %0, %1, %2" : "=v"(r) : "v"(o), "s"(base) : "memory");
  return r;
}
typedef __bf16 bf16x2 __attribute__((ext_vector_type(2)));
__device__ __forceinline__ float dot2(unsigned a, unsigned b, float c) {
  return __builtin_amdgcn_fdot2_f32_bf16(__builtin_bit_cast(bf16x2, a), __builtin_bit_cast(bf16x2, b), c, false);
}
__device__ __forceinline__ unsigned plo(unsigned hi, unsigned lo) { return __builtin_amdgcn_perm(hi, lo, 0x05040100u); }
__device__ __forceinline__ unsigned phi(unsigned hi, unsigned lo) { return __builtin_amdgcn_perm(hi, lo, 0x07060302u); }
__device__ __forceinline__ f32x4 mfma(const v4u& a, const v4u& b, f32x4 c) {
  return __builtin_amdgcn_mfma_f32_16x16x32_bf16(__builtin_bit_cast(bf16x8, a), __builtin_bit_cast(bf16x8, b), c, 0, 0, 0);
}
__device__ __forceinline__ unsigned now() { return (unsigned)__builtin_amdgcn_s_memrealtime(); }
__device__ __forceinline__ void wait_until(unsigned t) {
#pragma unroll 1
  for (int n = 0; n < 4096; ++n) {
    if ((int)(now() - t) >= 0) break;
    __builtin_amdgcn_s_sleep(1);
  }
}
__device__ __forceinline__ float gate_of(const v4u& xa, const v4u& ga) {
  float s = dot2(xa.x, ga.x, dot2(xa.y, ga.y, 0.f)) + dot2(xa.z, ga.z, dot2(xa.w, ga.w, 0.f));
  s = xrow_sum(row_sum(s));
  return 1.f / (1.f + __expf(-s));
}
__device__ __forceinline__ unsigned swz(unsigned row) { return ((row & 3) << 1) | ((row >> 3) & 1) | (((row >> 2) ^ (row >> 3)) & 1) << 3; }
__device__ __forceinline__ unsigned lds_off(unsigned row, unsigned ch) { return 1024u * row + 16u * (ch ^ swz(row)); }

constexpr unsigned QSB = 1056, PSB = 288;
constexpr unsigned L_AUX = 131072;
constexpr unsigned L_QB = L_AUX, L_PL = L_QB + 16 * QSB, L_WM = L_PL + 16 * PSB, L_FS = L_WM + 512, L_END = L_FS + 1024;
constexpr unsigned L_QP = L_AUX, L_LB = L_QP + 512, L_CB = L_LB + 16 * PSB, L_ST = L_CB + 8 * PSB;
constexpr unsigned L_UV = 98304;

__global__ __launch_bounds__(NT, 2) void k3_mix(const u16* __restrict__ x, const u16* __restrict__ c_kv,
                                               const u16* __restrict__ w_q, const u16* __restrict__ w_gate,
                                               const u16* __restrict__ w_up,
                                               const u16* __restrict__ w_uk, const u16* __restrict__ w_uv,
                                               const u16* __restrict__ w_o, const u16* __restrict__ w_down,
                                               const u16* __restrict__ gate_w, unsigned char* __restrict__ ws,
                                               u16* __restrict__ out) {
  __shared__ __attribute__((aligned(1024))) unsigned char sm[L_END];
  const unsigned sb = (unsigned)(size_t)sm;
  const int t = threadIdx.x, lane = t & 63, w = __builtin_amdgcn_readfirstlane(t >> 6);
  const int b = blockIdx.x;
  const int xcd = b & 7, idx = b >> 3;

  Tag T;
  {
    const u64 M44 = (1ull << 44) - 1;
    u64 u = (llvm_dispatch_id() & 0xffffffull) | ((((u64)__builtin_amdgcn_dispatch_ptr() >> 12) & 0xfffffull) << 24);
    u = (u + 0x51f42d4c95ull) & M44;
    u ^= u >> 21; u = (u * 0x545f4914f6dull) & M44;
    u ^= u >> 23; u = (u * 0xe3779b97f4bull) & M44;
    u ^= u >> 22;
    T.t0 = (unsigned)u & 0x7ffu; T.t1 = (unsigned)(u >> 11) & 0x7ffu; T.t2 = (unsigned)(u >> 22) & 0x7ffu; T.t3 = (unsigned)(u >> 33) & 0x7ffu;
  }
  asm volatile("" :: "s"(x), "s"(c_kv), "s"(w_q), "s"(w_gate), "s"(w_up), "s"(w_uk), "s"(w_uv), "s"(w_o), "s"(w_down), "s"(gate_w), "s"(ws), "s"(out),
                     "s"(T.t0), "s"(T.t1), "s"(T.t2), "s"(T.t3));
  const __amdgpu_buffer_rsrc_t rsw = __builtin_amdgcn_make_buffer_rsrc((void*)ws, 0, (int)WS_END, 0x00020000);
  const unsigned t0 = now();
  unsigned amp = 0;
  u16x4 vt[32];
  unsigned tra0, tra1;
  {
    const unsigned gq = lane >> 4, q = (lane & 15) >> 2, p = lane & 3;
    tra0 = sb + lds_off(8 * gq + q, 8 * w + (p >> 1)) + 8 * (p & 1);
    tra1 = sb + lds_off(8 * gq + 4 + q, 8 * w + (p >> 1)) + 8 * (p & 1);
  }
  auto trread = [&](int nt) {
    const unsigned ad0 = tra0 ^ (32u * nt), ad1 = tra1 ^ (32u * nt);
    const unsigned ad2 = ad0 + 65536u, ad3 = ad1 + 65536u;
    asm volatile("ds_read_b64_tr_b16 %0, %8\n\tds_read_b64_tr_b16 %1, %9\n\tds_read_b64_tr_b16 %2, %8 offset:32768\n\tds_read_b64_tr_b16 %3, %9 offset:32768\n\t"
                 "ds_read_b64_tr_b16 %4, %10\n\tds_read_b64_tr_b16 %5, %11\n\tds_read_b64_tr_b16 %6, %10 offset:32768\n\tds_read_b64_tr_b16 %7, %11 offset:32768"
                 : "=&v"(vt[nt * 8]), "=&v"(vt[nt * 8 + 1]), "=&v"(vt[nt * 8 + 2]), "=&v"(vt[nt * 8 + 3]), "=&v"(vt[nt * 8 + 4]), "=&v"(vt[nt * 8 + 5]), "=&v"(vt[nt * 8 + 6]), "=&v"(vt[nt * 8 + 7])
                 : "v"(ad0), "v"(ad1), "v"(ad2), "v"(ad3) : "memory");
  };
  auto vfrag = [&](int nt, int ks) {
    const v2u a0 = __builtin_bit_cast(v2u, vt[nt * 8 + 2 * ks]), a1 = __builtin_bit_cast(v2u, vt[nt * 8 + 2 * ks + 1]);
    return v4u{a0.x, a0.y, a1.x, a1.y};
  };
  auto tile_rows = [&](const u16* src, int i0, int i1, int w) {
    const unsigned cbase = sb + w * 16384;
    const unsigned l16 = lane * 16;
#pragma unroll
    for (int i = i0; i < i1; i += 4)
      glds16x4(src + (w * 16 + i) * 512, l16 ^ (swz(i) * 16), l16 ^ (swz(i + 1) * 16), l16 ^ (swz(i + 2) * 16), l16 ^ (swz(i + 3) * 16), cbase + i * 1024);
  };

  auto q_partial = [&]() {
    const int hq = MAPQ ? idx : xcd * 4 + (idx & 3), sq = MAPQ ? xcd : idx >> 2;
    v4u wq[4];
    unsigned xr[4];
    const unsigned qa0 = (((sq * 64 + (lane >> 2)) * H + hq) * D + w * 32 + (lane & 3) * 8) * 2, xa = (sq * 64 + (lane >> 2)) * 2;
    const unsigned qa1 = qa0 + 131072, qa2 = qa0 + 262144, qa3 = qa0 + 393216;
    asm volatile("global_load_dwordx4 %0, %8, %13\n\tglobal_load_dwordx4 %1, %9, %13\n\tglobal_load_dwordx4 %2, %10, %13\n\tglobal_load_dwordx4 %3, %11, %13\n\t"
                 "global_load_ushort %4, %12, %14\n\tglobal_load_ushort %5, %12, %14 offset:32\n\tglobal_load_ushort %6, %12, %14 offset:64\n\tglobal_load_ushort %7, %12, %14 offset:96"
                 : "=&v"(wq[0]), "=&v"(wq[1]), "=&v"(wq[2]), "=&v"(wq[3]), "=&v"(xr[0]), "=&v"(xr[1]), "=&v"(xr[2]), "=&v"(xr[3])
                 : "v"(qa0), "v"(qa1), "v"(qa2), "v"(qa3), "v"(xa), "s"(w_q), "s"(x) : "memory");
    int qo = (int)(WS_QP + ((hq * 8 + sq) * D + w * 32 + (lane & 3) * 8 + (lane >> 5) * 4) * 4);
    asm volatile("s_waitcnt vmcnt(0)" : "+v"(wq[0]), "+v"(wq[1]), "+v"(wq[2]), "+v"(wq[3]), "+v"(xr[0]), "+v"(xr[1]), "+v"(xr[2]), "+v"(xr[3]), "+v"(qo) :: "memory");
    const unsigned x01 = xr[0] | (xr[1] << 16), x23 = xr[2] | (xr[3] << 16);
    float acc[8], b4[4];
#pragma unroll
    for (int k = 0; k < 4; ++k) {
      acc[2 * k] = dot2(plo(wq[3][k], wq[2][k]), x23, dot2(plo(wq[1][k], wq[0][k]), x01, 0.f));
      acc[2 * k + 1] = dot2(phi(wq[3][k], wq[2][k]), x23, dot2(phi(wq[1][k], wq[0][k]), x01, 0.f));
    }
#pragma unroll
    for (int k = 0; k < 4; ++k) {
      float v = sw32(acc[k], acc[k + 4]);
      v += dppf<0x128>(v); v += dppf<0x124>(v);
      b4[k] = sw16(v, v);
    }
    if ((lane & 28) == 0) __builtin_amdgcn_raw_buffer_store_b128(enc(T, b4[0], b4[1], b4[2], b4[3]), rsw, qo, 0, 16);
  };

  if (__builtin_expect(idx < 16, 1)) {
    const int h = xcd * 4 + (idx & 3), j = idx >> 2;
    const int src = xcd * 16 + idx;
    v2u ur[8];
    auto uv_load = [&]() {
      const unsigned c0 = j * 128 + ((w >> 1) & 1) * 64 + (w & 1) * 8 + (lane >> 4) * 2;
      const unsigned uo = (c0 * 4096 + h * 128 + (w >> 2) * 64 + (lane & 15) * 4) * 2;
#pragma unroll
      for (int i = 0; i < 8; ++i)
        asm volatile("global_load_dwordx2 %0, %1, %2" : "=v"(ur[i]) : "v"(uo), "s"(w_uv + ((i & 3) * 16 + (i >> 2)) * 4096) : "memory");
    };
    if (w < 4) {
      q_partial();
      STAMP(1);
      v4u qg = {0u, 0u, 0u, 0u};
      const unsigned oq = (unsigned)(WS_QP + ((h * 8 + (lane >> 3)) * D + w * 32 + (lane & 7) * 4) * 4);
      if (PRE_Q) __builtin_amdgcn_s_sleep(PRE_Q);
      if (TQ) wait_until(t0 + TQ);
      u64 m = ~0ull;
      unsigned n = 0;
      for (;;) {
        poll1(qg, oq, ws, m);
        m = stale(chk(T, 0u, qg));
        if (m == 0ull || ++n > SPIN_CAP) break;
        __builtin_amdgcn_s_sleep(SLP);
      }
      SPINS(1, 0, n);
      const f32x4 q4 = pay(qg);
      float qv = sw16(sw32(q4[0], q4[2]), sw32(q4[1], q4[3]));
      qv += dppf<0x128>(qv);
      *(float*)(sm + L_QP + (w * 32 + (lane & 7) * 4 + (lane >> 4)) * 4) = qv;
      WG_BARRIER();
      STAMP(2);
    } else {
      v4u wk[8];
      const unsigned ko = ((j * 128 + (w - 4) * 32 + (lane >> 4) * 8) * 4096 + h * 128 + (lane & 15) * 8) * 2;
#pragma unroll
      for (int i = 0; i < 8; ++i) wk[i] = aload(w_uk + i * 4096, ko);
      int qso = (int)(WS_QA + (h * DC + j * 128 + (w - 4) * 32 + (lane >> 4) * 8 + ((lane >> 3) & 1) * 4) * 4);
      asm volatile("s_waitcnt vmcnt(0)" : "+v"(wk[0]), "+v"(wk[1]), "+v"(wk[2]), "+v"(wk[3]), "+v"(wk[4]), "+v"(wk[5]), "+v"(wk[6]), "+v"(wk[7]), "+v"(qso) :: "memory");
      f32x2 wf[32];
#pragma unroll
      for (int i = 0; i < 8; ++i)
#pragma unroll
        for (int k = 0; k < 4; ++k) wf[4 * i + k] = f32x2{bf_lo(wk[i][k]), bf_hi(wk[i][k])};
#pragma unroll
      for (int i = 0; i < 32; i += 8)
        asm volatile("" : "+v"(wf[i]), "+v"(wf[i + 1]), "+v"(wf[i + 2]), "+v"(wf[i + 3]), "+v"(wf[i + 4]), "+v"(wf[i + 5]), "+v"(wf[i + 6]), "+v"(wf[i + 7]));
      STAMPW(22, 256);
      __builtin_amdgcn_s_barrier();
      asm volatile("" ::: "memory");
      f32x4 q0, q1;
      asm volatile("ds_read_b128 %0, %2\n\tds_read_b128 %1, %2 offset:16\n\ts_waitcnt lgkmcnt(0)" : "=&v"(q0), "=&v"(q1) : "v"(sb + L_QP + (lane & 15) * 32) : "memory");
      const float scale = 0.08838834764831845f * 1.4426950408889634f;
      const f32x2 qa = {q0[0], q0[1]}, qb = {q0[2], q0[3]}, qc = {q1[0], q1[1]}, qd = {q1[2], q1[3]};
      float v[8], r4[4];
#pragma unroll
      for (int i = 0; i < 8; ++i) {
        const f32x2 a = (qa * wf[4 * i] + qb * wf[4 * i + 1]) + (qc * wf[4 * i + 2] + qd * wf[4 * i + 3]);
        v[i] = a[0] + a[1];
      }
#pragma unroll
      for (int i = 0; i < 4; ++i) {
        float e;
        asm("s_nop 1\n\tv_add_f32_dpp %0, %1, %1 row_ror:8 row_mask:0xf bank_mask:0x3\n\tv_add_f32_dpp %0, %2, %2 row_ror:8 row_mask:0xf bank_mask:0xc\n\ts_nop 1"
            : "=&v"(e) : "v"(v[i]), "v"(v[i + 4]));
        e += dppf<0xB1>(e); e += dppf<0x4E>(e); e += dppf<0x141>(e);
        r4[i] = e * scale;
      }
      if ((lane & 7) == 0) __builtin_amdgcn_raw_buffer_store_b128(enc(T, r4[0], r4[1], r4[2], r4[3]), rsw, qso, 0, 16);
      STAMPW(3, 256);
    }
    __builtin_amdgcn_s_barrier();
    if (UVP == 1) uv_load();
    if (TSD) __builtin_amdgcn_s_sleep(TSD);
    if (TS) wait_until(t0 + TS);
    v4u xa = aload(x, lane * 16), ga = aload(gate_w, lane * 16);
    if (UVP == 0) uv_load();
    tile_rows(w_o + (size_t)h * D * MD, 0, 16, ROTW ? (w + 2 * j) & 7 : w);
    STAMP(23);
    asm volatile("s_waitcnt vmcnt(16)" : "+v"(xa), "+v"(ga), "+v"(ur[0]), "+v"(ur[1]), "+v"(ur[2]), "+v"(ur[3]), "+v"(ur[4]), "+v"(ur[5]), "+v"(ur[6]), "+v"(ur[7]) :: "memory");
    const float gatev = gate_of(xa, ga);
    v4u ub[4];
#pragma unroll
    for (int k = 0; k < 4; ++k) {
      ub[0][k] = plo(ur[2 * k + 1].x, ur[2 * k].x); ub[1][k] = phi(ur[2 * k + 1].x, ur[2 * k].x);
      ub[2][k] = plo(ur[2 * k + 1].y, ur[2 * k].y); ub[3][k] = phi(ur[2 * k + 1].y, ur[2 * k].y);
    }
    asm volatile("" : "+v"(ub[0]), "+v"(ub[1]), "+v"(ub[2]), "+v"(ub[3]));
    VMWAIT(0);
    __builtin_amdgcn_s_barrier();
#pragma unroll
    for (int nt = 0; nt < 4; ++nt) trread(nt);
    STAMP(4);
    const unsigned unit = h * 4 + j;
    const unsigned po = (unsigned)(WS_PART + (unit * NR + w * 8) * 512 + lane * 16);
    const unsigned mo = (unsigned)(WS_ML + (((h >> 3) * NR + w * 8 + 2 * (lane & 3) + (lane >> 5)) * 8 + (h & 7)) * 16);
    {
      v4u an = {0u, 0u, 0u, 0u};
      const unsigned ao = (unsigned)(WS_ANN + ((h >> 4) * NR + w * 8 + (lane & 7)) * 128);
      unsigned n = 0;
      for (;;) {
        poll1(an, ao, ws, ~0ull);
        if (stale(chk(T, 0u, an)) == 0ull || ++n > SPIN_CAP) break;
        __builtin_amdgcn_s_sleep(2);
      }
      const unsigned tn = now();
      int dlt = (int)(an.x - (tn << 12)) >> 12;
      dlt = max(dlt, (int)dppu<0xB1>((unsigned)dlt)); dlt = max(dlt, (int)dppu<0x4E>((unsigned)dlt)); dlt = max(dlt, (int)dppu<0x141>((unsigned)dlt));
      STAMP(5);
      wait_until(tn + __builtin_amdgcn_readfirstlane(dlt) + DB);
    }
    v4u pb[4], ml = {0u, 0u, 0u, 0u};
#pragma unroll
    for (int i = 0; i < 4; ++i) pb[i] = v4u{0u, 0u, 0u, 0u};
    {
      u64 m = ~0ull;
      unsigned n = 0;
      for (;;) {
        poll5(pb, po, ml, mo, ws, m);
        unsigned bad = chk(T, 0u, ml);
#pragma unroll
        for (int i = 0; i < 4; ++i) bad = chk(T, bad, pb[i]);
        m = stale(bad);
        if (m == 0ull || ++n > SPIN_CAP) break;
        __builtin_amdgcn_s_sleep(SLP);
      }
      SPINS(3, 0, n);
    }
    STAMP(6); DW(1);
    {
      const f32x4 st = pay(ml);
      float Mw = vmax(st[0], dppf<0xB1>(st[0]));
      Mw = vmax(Mw, dppf<0x4E>(Mw));
      auto s = __builtin_amdgcn_permlane32_swap(asu(Mw), asu(Mw), false, false);
      Mw = vmax(asf(s[0]), asf(s[1]));
      const float e = __builtin_amdgcn_exp2f(st[0] - Mw);
      float lw = e * (st[1] + st[2]);
      lw += dppf<0xB1>(lw); lw += dppf<0x4E>(lw);
      lw = sw32(lw, lw);
      const f32x4 a4 = (dppf<0x00>(e) * pay(pb[0]) + dppf<0x55>(e) * pay(pb[1])) + (dppf<0xAA>(e) * pay(pb[2]) + dppf<0xFF>(e) * pay(pb[3]));
      const float v0 = sw32(a4[0], a4[2]), v1 = sw32(a4[1], a4[3]);
      const unsigned hi = pack2(v0, v1), lo = pack2(v0 - bf_lo(hi), v1 - bf_hi(hi));
      unsigned char* lp = sm + L_LB + (2 * w) * PSB + (lane & 31) * 8 + (lane >> 5) * 4;
      *(unsigned*)lp = hi;
      *(unsigned*)(lp + PSB) = lo;
      if (lane == 0) { *(float*)(sm + L_ST + w * 4) = Mw; *(float*)(sm + L_ST + 32 + w * 4) = lw; }
    }
    WG_BARRIER();
    STAMP(7);
    {
      const v4u a = *(const v4u*)(sm + L_LB + (lane & 15) * PSB + (w & 3) * 64 + (lane >> 4) * 16);
      const float ms = *(const float*)(sm + L_ST + (lane & 7) * 4), ls = *(const float*)(sm + L_ST + 32 + (lane & 7) * 4);
      const v2u mp = *(const v2u*)(sm + L_ST + (lane >> 4) * 8);
      const f32x4 z = {0.f, 0.f, 0.f, 0.f};
      f32x4 dd[4];
#pragma unroll
      for (int jj = 0; jj < 4; ++jj) dd[jj] = mfma(a, ub[jj], z);
      float M = vmax(ms, dppf<0xB1>(ms)); M = vmax(M, dppf<0x4E>(M)); M = vmax(M, dppf<0x141>(M));
      float L = __builtin_amdgcn_exp2f(ms - M) * ls;
      L += dppf<0xB1>(L); L += dppf<0x4E>(L); L += dppf<0x141>(L);
      const float sc = gatev * __builtin_amdgcn_rcpf(L);
      const float f0 = __builtin_amdgcn_exp2f(asf(mp.x) - M) * sc, f1 = __builtin_amdgcn_exp2f(asf(mp.y) - M) * sc;
      float c4[4];
#pragma unroll
      for (int jj = 0; jj < 4; ++jj) c4[jj] = f0 * (dd[jj][0] + dd[jj][1]) + f1 * (dd[jj][2] + dd[jj][3]);
      const float cv = sw16(sw32(c4[0], c4[2]), sw32(c4[1], c4[3]));
      const u16 hi = f2bf(cv), lo = f2bf(cv - bf2f(hi));
      unsigned char* cp = sm + L_CB + (w & 3) * 2 * PSB + ((w >> 2) * 64 + (lane & 15) * 4 + (lane >> 4)) * 2;
      *(u16*)cp = hi;
      *(u16*)(cp + PSB) = lo;
    }
    WG_BARRIER();
    STAMP(8);
    {
      const unsigned char* cp = sm + L_CB + (lane & 7) * PSB + (lane >> 4) * 16;
      v4u pf[4];
#pragma unroll
      for (int ks = 0; ks < 4; ++ks) pf[ks] = *(const v4u*)(cp + ks * 64);
      f32x4 acc[4];
#pragma unroll
      for (int nt = 0; nt < 4; ++nt) acc[nt] = f32x4{0.f, 0.f, 0.f, 0.f};
#pragma unroll
      for (int ks = 0; ks < 4; ++ks)
#pragma unroll
        for (int nt = 0; nt < 4; ++nt) acc[nt] = mfma(vfrag(nt, ks), pf[ks], acc[nt]);
      float a[4];
#define HM(o, i, bm) "v_add_f32_dpp %" #o ", %" #i ", %" #i " row_half_mirror row_mask:0xf bank_mask:" #bm "\n\t"
      asm volatile("s_nop 15\n\ts_nop 3\n\t"
                   HM(0, 4, 0x1) HM(1, 5, 0x1) HM(2, 6, 0x1) HM(3, 7, 0x1) HM(0, 8, 0x2) HM(1, 9, 0x2) HM(2, 10, 0x2) HM(3, 11, 0x2)
                   HM(0, 12, 0x4) HM(1, 13, 0x4) HM(2, 14, 0x4) HM(3, 15, 0x4) HM(0, 16, 0x8) HM(1, 17, 0x8) HM(2, 18, 0x8) HM(3, 19, 0x8) "s_nop 1"
                   : "=&v"(a[0]), "=&v"(a[1]), "=&v"(a[2]), "=&v"(a[3])
                   : "v"(acc[0][0]), "v"(acc[0][1]), "v"(acc[0][2]), "v"(acc[0][3]), "v"(acc[1][0]), "v"(acc[1][1]), "v"(acc[1][2]), "v"(acc[1][3]),
                     "v"(acc[2][0]), "v"(acc[2][1]), "v"(acc[2][2]), "v"(acc[2][3]), "v"(acc[3][0]), "v"(acc[3][1]), "v"(acc[3][2]), "v"(acc[3][3]));
#pragma unroll
      for (int k = 0; k < 4; ++k) { a[k] += dppf<0xB1>(a[k]); a[k] += dppf<0x4E>(a[k]); }
      if ((lane & 3) == 0)
        __builtin_amdgcn_raw_buffer_store_b128(enc(T, a[0], a[1], a[2], a[3]), rsw, (int)(WS_FIN + src * 2048 + (w * 16 + (lane & 12) + (lane >> 4)) * 16), 0, 16);
    }
    STAMP(9); DW(2);
  } else {
    const int ai = idx - 16;
    const int r = xcd * 8 + (ai >> 1), g = ai & 1;
    const int fs = ((ai >> 2) * 8 + xcd) * 4 + (ai & 3);
    const bool has_ffn = fs < NFS;
    const u16* ct = c_kv + (size_t)r * KPR * DC;
    if (w < 4) {
      q_partial();
      STAMP(10);
      __builtin_amdgcn_s_barrier();
      tile_rows(ct, 0, 16, w);
    } else {
      if (WDELAY) __builtin_amdgcn_s_sleep(WDELAY);
      tile_rows(ct, 0, B1, w);
      __builtin_amdgcn_s_barrier();
      tile_rows(ct, B1, 16, w);
    }
    v4u afr[16];
    v4u ag[4];
    const unsigned qo = (unsigned)(WS_QA + (g * 16 + 2 * w) * 2048 + lane * 16);
    VMWAIT(PAW);
    if (TA) wait_until(t0 + TA);
    issue4(ag, qo, ws);
    STAMP(11);
    VMWAIT(4);
    STAMP(12);
    {
      const unsigned arow = sb + (w * 16 + (lane & 15)) * 1024u;
      const unsigned sw = swz(lane & 15), kq = lane >> 4;
#pragma unroll
      for (int i = 0; i < 16; i += 4) {
        const unsigned a0 = arow + 16u * ((i * 4 + kq) ^ sw), a1 = arow + 16u * ((i * 4 + 4 + kq) ^ sw);
        const unsigned a2 = arow + 16u * ((i * 4 + 8 + kq) ^ sw), a3 = arow + 16u * ((i * 4 + 12 + kq) ^ sw);
        asm volatile("ds_read_b128 %0, %4\n\tds_read_b128 %1, %5\n\tds_read_b128 %2, %6\n\tds_read_b128 %3, %7"
                     : "=&v"(afr[i]), "=&v"(afr[i + 1]), "=&v"(afr[i + 2]), "=&v"(afr[i + 3]) : "v"(a0), "v"(a1), "v"(a2), "v"(a3) : "memory");
      }
    }
    {
      unsigned n = 0;
      vw4<0>(ag);
      for (;;) {
        unsigned bad = 0u;
#pragma unroll
        for (int i = 0; i < 4; ++i) bad = chk(T, bad, ag[i]);
        const u64 m = stale(bad);
        if (m == 0ull || ++n > SPIN_CAP) break;
        __builtin_amdgcn_s_sleep(SLP);
        poll4(ag, qo, ws, m);
      }
      SPINS(2, 0, n);
    }
    STAMP(13); DW(3);
#pragma unroll
    for (int i = 0; i < 4; ++i) {
      const f32x4 a = pay(ag[i]);
      *(v2u*)(sm + L_QB + (2 * w + (i >> 1)) * QSB + ((i & 1) * 64 + lane) * 8) = v2u{pack2(a[0], a[1]), pack2(a[2], a[3])};
    }
    const int fu = (xcd * 16 + ai) * 6 + w;
    const bool ffn_p = w < 6 && fu < 688;
    const int fk = ffn_p ? fu / 43 : 0, fg = ffn_p ? fu - fk * 43 : 0;
    v4u wg[2], wu[2];
    unsigned xf[2];
    if (ffn_p) {
      const unsigned row = fk * 32 + (lane >> 2);
      const unsigned go = (row * FF + fg * 32 + (lane & 3) * 8) * 2;
      wg[0] = aload(w_gate, go); wu[0] = aload(w_up, go);
      wg[1] = aload(w_gate + 16 * FF, go); wu[1] = aload(w_up + 16 * FF, go);
      asm volatile("global_load_ushort %0, %2, %3\n\tglobal_load_ushort %1, %2, %3 offset:32" : "=&v"(xf[0]), "=&v"(xf[1]) : "v"(row * 2), "s"(x) : "memory");
    }
    unsigned a_b = sb + L_QB + (lane & 15) * QSB + (lane >> 4) * 16;
    unsigned a_mw = sb + L_WM + ((lane & 15) * 8 + w) * 4;
    unsigned a_mr = sb + L_WM + (lane & 15) * 32;
    unsigned a_pw = sb + L_PL + (lane & 15) * PSB + w * 32 + (lane >> 4) * 8;
    unsigned a_pr = sb + L_PL + (lane & 15) * PSB + (lane >> 4) * 16;
    int o_pv = (int)(WS_PART + (((g * 16 + (lane >> 4) * 4) * 4 + (w >> 1)) * NR + r) * 512 + (w & 1) * 256 + (lane & 15) * 16);
    int o_ml = (int)(WS_ML + (((g * 2 + ((lane >> 3) & 1)) * NR + r) * 8 + (lane & 7)) * 16);
    asm volatile("" : "+v"(a_b), "+v"(a_mw), "+v"(a_mr), "+v"(a_pw), "+v"(a_pr), "+v"(o_pv), "+v"(o_ml), "+v"(tra0), "+v"(tra1));
    WG_BARRIER();
    STAMP(14);
    if (w == 7) {
      const unsigned tn = now() << 12;
      if (lane < 8) __builtin_amdgcn_raw_buffer_store_b128(v4u{tn | T.t0, T.t1, T.t2, T.t3}, rsw, (int)(WS_ANN + (g * NR + r) * 128 + lane * 16), 0, 16);
    }
    f32x4 sacc;
    {
      v4u bfr[16];
      asm volatile("ds_read_b128 %0, %8\n\tds_read_b128 %1, %8 offset:64\n\tds_read_b128 %2, %8 offset:128\n\tds_read_b128 %3, %8 offset:192\n\t"
                   "ds_read_b128 %4, %8 offset:256\n\tds_read_b128 %5, %8 offset:320\n\tds_read_b128 %6, %8 offset:384\n\tds_read_b128 %7, %8 offset:448"
                   : "=&v"(bfr[0]), "=&v"(bfr[1]), "=&v"(bfr[2]), "=&v"(bfr[3]), "=&v"(bfr[4]), "=&v"(bfr[5]), "=&v"(bfr[6]), "=&v"(bfr[7])
                   : "v"(a_b) : "memory");
      asm volatile("ds_read_b128 %0, %8 offset:512\n\tds_read_b128 %1, %8 offset:576\n\tds_read_b128 %2, %8 offset:640\n\tds_read_b128 %3, %8 offset:704\n\t"
                   "ds_read_b128 %4, %8 offset:768\n\tds_read_b128 %5, %8 offset:832\n\tds_read_b128 %6, %8 offset:896\n\tds_read_b128 %7, %8 offset:960"
                   : "=&v"(bfr[8]), "=&v"(bfr[9]), "=&v"(bfr[10]), "=&v"(bfr[11]), "=&v"(bfr[12]), "=&v"(bfr[13]), "=&v"(bfr[14]), "=&v"(bfr[15])
                   : "v"(a_b) : "memory");
      f32x4 s0 = {0.f, 0.f, 0.f, 0.f}, s1 = {0.f, 0.f, 0.f, 0.f};
#pragma unroll
      for (int q = 0; q < 4; ++q) {
        asm volatile("s_waitcnt lgkmcnt(%6)" : "+v"(bfr[4 * q]), "+v"(bfr[4 * q + 1]), "+v"(bfr[4 * q + 2]), "+v"(bfr[4 * q + 3]), "+v"(s0), "+v"(s1) : "n"(12 - 4 * q) : "memory");
        if (q == 3) { trread(0); trread(1); }
        s0 = mfma(afr[4 * q], bfr[4 * q], s0);
        s1 = mfma(afr[4 * q + 1], bfr[4 * q + 1], s1);
        s0 = mfma(afr[4 * q + 2], bfr[4 * q + 2], s0);
        s1 = mfma(afr[4 * q + 3], bfr[4 * q + 3], s1);
      }
      sacc = s0 + s1;
    }
    {
      const float mx = xrow_vmax(vmax(vmax(sacc[0], sacc[1]), vmax(sacc[2], sacc[3])));
      if (lane < 16) asm volatile("ds_write_b32 %0, %1" :: "v"(a_mw), "v"(mx) : "memory");
    }
    STAMP(15);
    WG_BARRIER();
    float mref;
    {
      f32x4 m0, m1;
      asm volatile("ds_read_b128 %0, %2\n\tds_read_b128 %1, %2 offset:16" : "=&v"(m0), "=&v"(m1) : "v"(a_mr) : "memory");
      trread(2);
      asm volatile("s_waitcnt lgkmcnt(8)" : "+v"(m0), "+v"(m1) :: "memory");
      const float mx = vmax(vmax(vmax(m0[0], m0[1]), vmax(m0[2], m0[3])), vmax(vmax(m1[0], m1[1]), vmax(m1[2], m1[3])));
      mref = asf(encw(mx, T.t0));
      const v2u pp = {pack2(__builtin_amdgcn_exp2f(sacc[0] - mref), __builtin_amdgcn_exp2f(sacc[1] - mref)),
                      pack2(__builtin_amdgcn_exp2f(sacc[2] - mref), __builtin_amdgcn_exp2f(sacc[3] - mref))};
      asm volatile("ds_write_b64 %0, %1" :: "v"(a_pw), "v"(pp) : "memory");
    }
    trread(3);
    asm volatile("s_waitcnt lgkmcnt(8)" ::: "memory");
    __builtin_amdgcn_s_barrier();
    asm volatile("" ::: "memory");
    STAMP(16);
    {
      v4u pf[4];
      asm volatile("ds_read_b128 %0, %4\n\tds_read_b128 %1, %4 offset:64\n\tds_read_b128 %2, %4 offset:128\n\tds_read_b128 %3, %4 offset:192\n\ts_waitcnt lgkmcnt(0)"
                   : "=&v"(pf[0]), "=&v"(pf[1]), "=&v"(pf[2]), "=&v"(pf[3]) : "v"(a_pr) : "memory");
      f32x4 acc[4];
#pragma unroll
      for (int nt = 0; nt < 4; ++nt) acc[nt] = f32x4{0.f, 0.f, 0.f, 0.f};
#pragma unroll
      for (int ks = 0; ks < 4; ++ks)
#pragma unroll
        for (int nt = 0; nt < 4; ++nt) acc[nt] = mfma(pf[ks], vfrag(nt, ks), acc[nt]);
      if (w == 6) {
        const unsigned one = 0x3f803f80u;
        float l = 0.f;
#pragma unroll
        for (int ks = 0; ks < 4; ++ks) l += dot2(pf[ks].x, one, dot2(pf[ks].y, one, 0.f)) + dot2(pf[ks].z, one, dot2(pf[ks].w, one, 0.f));
        l = xrow_sum(l);
        const float lh = asf(encw(l, T.t1));
        if (lane < 16) __builtin_amdgcn_raw_buffer_store_b128(v4u{asu(mref), asu(lh), encw(l - lh, T.t2), T.t3}, rsw, o_ml, 0, 16);
      }
#pragma unroll
      for (int k = 0; k < 4; ++k)
        __builtin_amdgcn_raw_buffer_store_b128(enc(T, acc[0][k], acc[1][k], acc[2][k], acc[3][k]), rsw, o_pv + k * (4 * NR * 512), 0, 16);
    }
    STAMP(17); DW(4);
    if (ffn_p) {
      asm volatile("s_waitcnt vmcnt(4)" : "+v"(wg[0]), "+v"(wg[1]), "+v"(wu[0]), "+v"(wu[1]), "+v"(xf[0]), "+v"(xf[1]) :: "memory");
      const unsigned xp = xf[0] | (xf[1] << 16);
      float acc[16];
#pragma unroll
      for (int k = 0; k < 4; ++k) {
        acc[4 * k] = dot2(plo(wg[1][k], wg[0][k]), xp, 0.f);
        acc[4 * k + 1] = dot2(plo(wu[1][k], wu[0][k]), xp, 0.f);
        acc[4 * k + 2] = dot2(phi(wg[1][k], wg[0][k]), xp, 0.f);
        acc[4 * k + 3] = dot2(phi(wu[1][k], wu[0][k]), xp, 0.f);
      }
      float h4[4];
#pragma unroll
      for (int k = 0; k < 4; ++k) {
        float v = sw16(sw32(acc[k], acc[k + 8]), sw32(acc[k + 4], acc[k + 12]));
        v += dppf<0x128>(v); v += dppf<0x124>(v);
        h4[k] = v;
      }
      if ((lane & 12) == 0)
        __builtin_amdgcn_raw_buffer_store_b128(enc(T, h4[0], h4[1], h4[2], h4[3]), rsw, (int)(WS_FP + (fk * 688 + fg * 16 + (lane & 3) * 4 + (lane >> 4)) * 16), 0, 16);
      STAMP(18);
    }
    if (has_ffn) {
      const unsigned dofs = ((fs * 16 + (lane >> 3) * 2) * MD + w * 64 + (lane & 7) * 8) * 2;
      v4u d0 = aload(w_down, dofs), d1 = aload(w_down + MD, dofs), xa = aload(x, lane * 16), ga = aload(gate_w, lane * 16);
      v4u fp[2] = {v4u{0u, 0u, 0u, 0u}, v4u{0u, 0u, 0u, 0u}};
      const unsigned fo = (unsigned)(WS_FP + ((lane & 7) * 688 + fs * 8 + (lane >> 3)) * 16);
      if (PRE_F) __builtin_amdgcn_s_sleep(PRE_F);
      u64 m = ~0ull;
      unsigned n = 0;
      for (;;) {
        poll2(fp[0], fo, m, fp[1], fo + 8 * 688 * 16, m, ws);
        m = stale(chk(T, chk(T, 0u, fp[0]), fp[1]));
        if (m == 0ull || ++n > SPIN_CAP) break;
        __builtin_amdgcn_s_sleep(SLPC);
      }
      SPINS(4, 0, n);
      asm volatile("" : "+v"(d0), "+v"(d1), "+v"(xa), "+v"(ga) :: "memory");
      f32x4 s4 = pay(fp[0]) + pay(fp[1]);
#pragma unroll
      for (int k = 0; k < 4; ++k) { s4[k] += dppf<0xB1>(s4[k]); s4[k] += dppf<0x4E>(s4[k]); s4[k] += dppf<0x141>(s4[k]); }
      const float og = 1.f - gate_of(xa, ga);
      const float h0 = s4[0] / (1.f + __expf(-s4[0])) * s4[1] * og, h1 = s4[2] / (1.f + __expf(-s4[2])) * s4[3] * og;
      float f0[8], f1[8];
      unpack8(d0, f0); unpack8(d1, f1);
      float b4[4];
#pragma unroll
      for (int k = 0; k < 4; ++k) {
        float v = sw32(h0 * f0[k] + h1 * f1[k], h0 * f0[k + 4] + h1 * f1[k + 4]);
        v = sw16(v, v);
        b4[k] = v + dppf<0x128>(v);
      }
      if ((lane & 24) == 0)
        __builtin_amdgcn_raw_buffer_store_b128(enc(T, b4[0], b4[1], b4[2], b4[3]), rsw, (int)(WS_FIN + (128 + fs) * 2048 + (w * 16 + (lane & 7) * 2 + (lane >> 5)) * 16), 0, 16);
      STAMP(19);
    }
    if (w == 7) {
      const int k = xcd * 16 + ai;
      const unsigned fo = (unsigned)(WS_FIN + lane * 2048 + k * 16);
      v4u fv[4];
      fv[0] = fv[1] = fv[2] = v4u{0u, 0u, 0u, 0u};
      fv[3] = v4u{T.t0, T.t1, T.t2, T.t3};
      unsigned n = 0;
      for (;;) {
        poll2(fv[0], (unsigned)(WS_ANN + lane * 128), ~0ull, fv[1], (unsigned)(WS_ANN + 8192 + lane * 128), ~0ull, ws);
        if (stale(chk(T, chk(T, 0u, fv[0]), fv[1])) == 0ull || ++n > SPIN_CAP) break;
        __builtin_amdgcn_s_sleep(4);
      }
      const unsigned tn = now();
      int dlt = max((int)(fv[0].x - (tn << 12)) >> 12, (int)(fv[1].x - (tn << 12)) >> 12);
      dlt = max(dlt, (int)dppu<0xB1>((unsigned)dlt)); dlt = max(dlt, (int)dppu<0x4E>((unsigned)dlt)); dlt = max(dlt, (int)dppu<0x141>((unsigned)dlt));
      dlt = max(dlt, (int)dppu<0x128>((unsigned)dlt));
      dlt = max(max(__builtin_amdgcn_readlane(dlt, 0), __builtin_amdgcn_readlane(dlt, 16)), max(__builtin_amdgcn_readlane(dlt, 32), __builtin_amdgcn_readlane(dlt, 48)));
      const unsigned tc = tn + dlt + DC_;
      wait_until(tc - 80);
      u64 m = ~0ull;
      n = 0;
      for (;;) {
        poll2(fv[2], fo + 262144, m, fv[3], fo + 393216, m & ((1ull << (NFS - 64)) - 1), ws);
        m = stale(chk(T, chk(T, 0u, fv[2]), fv[3]));
        if (m == 0ull || ++n > SPIN_CAP) break;
        __builtin_amdgcn_s_sleep(SLPC);
      }
      SPINS(6, 448, n);
      wait_until(tc);
      m = ~0ull;
      n = 0;
      for (;;) {
        poll2(fv[0], fo, m, fv[1], fo + 131072, m, ws);
        m = stale(chk(T, chk(T, 0u, fv[0]), fv[1]));
        if (m == 0ull || ++n > SPIN_CAP) break;
        __builtin_amdgcn_s_sleep(SLPC);
      }
      SPINS(5, 448, n);
      STAMPW(20, 448);
      const f32x4 s4 = (pay(fv[0]) + pay(fv[1])) + (pay(fv[2]) + pay(fv[3]));
      const float v = row_sum(sw16(sw32(s4[0], s4[2]), sw32(s4[1], s4[3])));
      if ((lane & 15) == 0) out[k * 4 + (lane >> 4)] = f2bf(v);
      STAMPW(21, 448);
    }
  }
  wait_until(t0 + 2000 + 10 * amp);
}

extern "C" void kernel_launch(void* const* d_in, const int* in_sizes, int n_in, void* d_out, int out_size,
                              void* d_ws, size_t ws_size, hipStream_t stream) {
  const u16* x = (const u16*)d_in[0];
  const u16* c_kv = (const u16*)d_in[1];
  const u16* w_q = (const u16*)d_in[2];
  const u16* w_uk = (const u16*)d_in[3];
  const u16* w_uv = (const u16*)d_in[4];
  const u16* w_o = (const u16*)d_in[5];
  const u16* w_gate = (const u16*)d_in[7];
  const u16* w_up = (const u16*)d_in[8];
  const u16* w_down = (const u16*)d_in[9];
  const u16* gate_w = (const u16*)d_in[10];
  unsigned char* ws = (unsigned char*)d_ws;
  k3_mix<<<NWG, NT, 0, stream>>>(x, c_kv, w_q, w_gate, w_up, w_uk, w_uv, w_o, w_down, gate_w, ws, (u16*)d_out);
}
```

```cpp
#include <hip/hip_runtime.h>
#include <hip/hip_bf16.h>


#define WDELAY 24
#define PRE_Q 12
#define PAW 4
#define TA 360
#define TSD 30
#define DB 185
#define DM 30
#define DC_ 280
#define PRE_F 16
#define SLP 2
#define SLPC 2
typedef unsigned short u16;
typedef unsigned v4u __attribute__((ext_vector_type(4)));
typedef unsigned v2u __attribute__((ext_vector_type(2)));
typedef float f32x4 __attribute__((ext_vector_type(4)));
typedef float f32x2 __attribute__((ext_vector_type(2)));
typedef short bf16x8 __attribute__((ext_vector_type(8)));
typedef unsigned short u16x4 __attribute__((ext_vector_type(4)));
typedef unsigned long long u64;

constexpr int H = 32, D = 128, MD = 512, DC = 512, SK = 8192, FF = 1376;
constexpr int NWG = 256, NT = 512;
constexpr int NR = 64, KPR = 128;
constexpr int NFS = FF / 16;

constexpr size_t WS_PART = 0;
constexpr size_t PART_BYTES = (size_t)H * 4 * NR * 128 * 2;
constexpr size_t WS_CK = WS_PART + PART_BYTES;
constexpr size_t CK_BYTES = (size_t)NR * 8 * H * 16;
constexpr size_t WS_M = WS_CK + CK_BYTES;
constexpr size_t M_BYTES = (size_t)H * NR * 16;
constexpr size_t WS_ANN = WS_M + M_BYTES;
constexpr size_t ANN_BYTES = (size_t)2 * NR * 128;
constexpr size_t WS_FIN = WS_ANN + ANN_BYTES;
constexpr size_t FIN_BYTES = (size_t)256 * 512 * 4;
constexpr size_t WS_QA = WS_FIN + FIN_BYTES;
constexpr size_t QA_BYTES = (size_t)H * DC * 4;
constexpr size_t WS_QP = WS_QA + QA_BYTES;
constexpr size_t QP_BYTES = (size_t)H * 8 * D * 4;
constexpr size_t WS_FP = WS_QP + QP_BYTES;
constexpr size_t FP_BYTES = (size_t)16 * (FF / 2) * 16;
constexpr size_t WS_END = WS_FP + FP_BYTES + 128;

constexpr unsigned SPIN_CAP = 1u << 20;

__device__ __forceinline__ float asf(unsigned u) { return __builtin_bit_cast(float, u); }
__device__ __forceinline__ unsigned asu(float f) { return __builtin_bit_cast(unsigned, f); }
__device__ __forceinline__ float bf_lo(unsigned u) { return __builtin_bit_cast(float, u << 16); }
__device__ __forceinline__ float bf_hi(unsigned u) { return __builtin_bit_cast(float, u & 0xffff0000u); }
__device__ __forceinline__ u16 f2bf(float f) {
  __bf16 t = (__bf16)f;
  return __builtin_bit_cast(u16, t);
}
typedef __bf16 bf16x2 __attribute__((ext_vector_type(2)));
__device__ __forceinline__ unsigned pack2(float a, float b) { return __builtin_bit_cast(unsigned, __builtin_convertvector(f32x2{a, b}, bf16x2)); }
extern "C" __device__ unsigned long long llvm_dispatch_id() __asm("llvm.amdgcn.dispatch.id");
struct Tag { unsigned t0, t1, t2, t3; };
__device__ __forceinline__ unsigned r20(float f) { return (asu(f) + 0x800u) & 0xfffff000u; }
__device__ __forceinline__ v4u enc(const Tag& T, float a, float b, float c, float d) {
  return v4u{r20(a) | T.t0, r20(b) | T.t1, r20(c) | T.t2, r20(d) | T.t3};
}
__device__ __forceinline__ unsigned chk(const Tag& T, unsigned bad, const v4u& v, v4u& d) {
  d = v4u{v.x ^ T.t0, v.y ^ T.t1, v.z ^ T.t2, v.w ^ T.t3};
  return (bad | d.x | d.y) | (d.z | d.w);
}
__device__ __forceinline__ v4u encm(const Tag& T, unsigned mk, const f32x4& a) {
  return v4u{((asu(a[0]) + 0x800u) & mk) | T.t0, ((asu(a[1]) + 0x800u) & mk) | T.t1, ((asu(a[2]) + 0x800u) & mk) | T.t2, ((asu(a[3]) + 0x800u) & mk) | T.t3};
}
__device__ __forceinline__ u64 stale(unsigned bad) { return __builtin_amdgcn_ballot_w64((bad & 0xfffu) != 0u); }
__device__ __forceinline__ f32x4 pay(const v4u& v) { return __builtin_bit_cast(f32x4, v); }

__device__ __forceinline__ void unpack8(const v4u& v, float* o) {
  o[0] = bf_lo(v.x); o[1] = bf_hi(v.x); o[2] = bf_lo(v.y); o[3] = bf_hi(v.y);
  o[4] = bf_lo(v.z); o[5] = bf_hi(v.z); o[6] = bf_lo(v.w); o[7] = bf_hi(v.w);
}
template <int CTRL> __device__ __forceinline__ float dppf(float v) {
  return __builtin_bit_cast(float, __builtin_amdgcn_mov_dpp(__builtin_bit_cast(int, v), CTRL, 0xf, 0xf, true));
}
template <int CTRL> __device__ __forceinline__ unsigned dppu(unsigned v) {
  return (unsigned)__builtin_amdgcn_mov_dpp((int)v, CTRL, 0xf, 0xf, true);
}
__device__ __forceinline__ float row_sum(float v) {
  v += dppf<0x128>(v); v += dppf<0x124>(v); v += dppf<0x122>(v); v += dppf<0x121>(v);
  return v;
}
__device__ __forceinline__ float sw32(float a, float b) {
  auto s = __builtin_amdgcn_permlane32_swap(asu(a), asu(b), false, false);
  return asf(s[0]) + asf(s[1]);
}
__device__ __forceinline__ float sw16(float a, float b) {
  auto s = __builtin_amdgcn_permlane16_swap(asu(a), asu(b), false, false);
  return asf(s[0]) + asf(s[1]);
}
__device__ __forceinline__ float xrow_sum(float v) { v = sw16(v, v); return sw32(v, v); }
__device__ __forceinline__ float vmax(float a, float b) { float r; asm("v_max_f32 %0, %1, %2" : "=v"(r) : "v"(a), "v"(b)); return r; }
__device__ __forceinline__ float vmax3(float a, float b, float c) { float r; asm("v_max3_f32 %0, %1, %2, %3" : "=v"(r) : "v"(a), "v"(b), "v"(c)); return r; }
__device__ __forceinline__ float xrow_vmax(float v) {
  auto a = __builtin_amdgcn_permlane16_swap(asu(v), asu(v), false, false);
  v = vmax(asf(a[0]), asf(a[1]));
  auto b = __builtin_amdgcn_permlane32_swap(asu(v), asu(v), false, false);
  return vmax(asf(b[0]), asf(b[1]));
}
__device__ __forceinline__ void glds16x4(const void* base, unsigned o0, unsigned o1, unsigned o2, unsigned o3, unsigned lds_dst) {
  unsigned keep;
  asm volatile("s_mov_b32 %0, m0\n\ts_mov_b32 m0, %6\n\ts_nop 0\n\tglobal_load_lds_dwordx4 %1, %5\n\tglobal_load_lds_dwordx4 %2, %5 offset:1024\n\t"
               "global_load_lds_dwordx4 %3, %5 offset:2048\n\tglobal_load_lds_dwordx4 %4, %5 offset:3072\n\ts_mov_b32 m0, %0"
               : "=&s"(keep) : "v"(o0), "v"(o1), "v"(o2), "v"(o3), "s"(base), "s"(lds_dst) : "memory");
}
#define WG_BARRIER() do { asm volatile("s_waitcnt lgkmcnt(0)" ::: "memory"); __builtin_amdgcn_s_barrier(); asm volatile("" ::: "memory"); } while (0)
#define SB __builtin_amdgcn_sched_barrier(0)
#define VMWAIT(n) asm volatile("s_waitcnt vmcnt(%0)" :: "n"(n) : "memory")

__device__ __forceinline__ void poll1(v4u& r, unsigned o, const void* ws, u64 m) {
  asm volatile("s_mov_b64 exec, %3\n\tglobal_load_dwordx4 %0, %1, %2 sc1\n\ts_mov_b64 exec, -1\n\ts_waitcnt vmcnt(0)"
               : "+v"(r) : "v"(o), "s"(ws), "s"(m) : "memory");
}
__device__ __forceinline__ void poll2(v4u& r0, unsigned o0, u64 m0, v4u& r1, unsigned o1, u64 m1, const void* ws) {
  asm volatile("s_mov_b64 exec, %5\n\tglobal_load_dwordx4 %0, %2, %4 sc1\n\ts_mov_b64 exec, %6\n\tglobal_load_dwordx4 %1, %3, %4 sc1\n\t"
               "s_mov_b64 exec, -1\n\ts_waitcnt vmcnt(0)"
               : "+v"(r0), "+v"(r1) : "v"(o0), "v"(o1), "s"(ws), "s"(m0), "s"(m1) : "memory");
}
#define LD4 "global_load_dwordx4 %0, %4, %5 sc1\n\tglobal_load_dwordx4 %1, %4, %5 offset:1024 sc1\n\t" \
            "global_load_dwordx4 %2, %4, %5 offset:2048 sc1\n\tglobal_load_dwordx4 %3, %4, %5 offset:3072 sc1"
__device__ __forceinline__ void issue4(v4u* r, unsigned o, const void* ws) {
  asm volatile(LD4 : "=&v"(r[0]), "=&v"(r[1]), "=&v"(r[2]), "=&v"(r[3]) : "v"(o), "s"(ws) : "memory");
}
__device__ __forceinline__ void poll4(v4u* r, unsigned o, const void* ws, u64 m) {
  asm volatile("s_mov_b64 exec, %6\n\t" LD4 "\n\ts_mov_b64 exec, -1\n\ts_waitcnt vmcnt(0)"
               : "+v"(r[0]), "+v"(r[1]), "+v"(r[2]), "+v"(r[3]) : "v"(o), "s"(ws), "s"(m) : "memory");
}
template <int C> __device__ __forceinline__ void vw4(v4u* r) { asm volatile("s_waitcnt vmcnt(%4)" : "+v"(r[0]), "+v"(r[1]), "+v"(r[2]), "+v"(r[3]) : "n"(C) : "memory"); }
__device__ __forceinline__ v4u aload(const void* base, unsigned o) {
  v4u r;
  asm volatile("global_load_dwordx4 %0, %1, %2" : "=v"(r) : "v"(o), "s"(base) : "memory");
  return r;
}
__device__ __forceinline__ float dot2(unsigned a, unsigned b, float c) {
  return __builtin_amdgcn_fdot2_f32_bf16(__builtin_bit_cast(bf16x2, a), __builtin_bit_cast(bf16x2, b), c, false);
}
__device__ __forceinline__ unsigned hkey(const Tag& T, bool s, unsigned wp) {
  unsigned v = (s ? (T.t1 >> 4) | (T.t2 << 8) | (T.t3 << 20) : T.t0 | (T.t1 << 12) | (T.t2 << 24)) + wp * 0x9e3779b9u;
  v ^= v >> 16; v *= 0x7feb352du; v ^= v >> 15; v *= 0x846ca68bu; v ^= v >> 16;
  return v | 0x00010001u;
}
__device__ __forceinline__ unsigned plo(unsigned hi, unsigned lo) { return __builtin_amdgcn_perm(hi, lo, 0x05040100u); }
__device__ __forceinline__ unsigned phi(unsigned hi, unsigned lo) { return __builtin_amdgcn_perm(hi, lo, 0x07060302u); }
__device__ __forceinline__ f32x4 mfma(const v4u& a, const v4u& b, f32x4 c) {
  return __builtin_amdgcn_mfma_f32_16x16x32_bf16(__builtin_bit_cast(bf16x8, a), __builtin_bit_cast(bf16x8, b), c, 0, 0, 0);
}
__device__ __forceinline__ unsigned now() { return (unsigned)__builtin_amdgcn_s_memrealtime(); }
__device__ __forceinline__ void wait_until(unsigned t) {
#pragma unroll 1
  for (int n = 0; n < 4096; ++n) {
    if ((int)(now() - t) >= 0) break;
    __builtin_amdgcn_s_sleep(1);
  }
}
__device__ __forceinline__ float gate_of(const v4u& xa, const v4u& ga) {
  float s = dot2(xa.x, ga.x, dot2(xa.y, ga.y, 0.f)) + dot2(xa.z, ga.z, dot2(xa.w, ga.w, 0.f));
  s = xrow_sum(row_sum(s));
  return 1.f / (1.f + __expf(-s));
}
__device__ __forceinline__ unsigned swz(unsigned row) { return ((row & 3) << 1) | ((row >> 3) & 1) | (((row >> 2) ^ (row >> 3)) & 1) << 3; }
__device__ __forceinline__ unsigned lds_off(unsigned row, unsigned ch) { return 1024u * row + 16u * (ch ^ swz(row)); }

constexpr unsigned QSB = 1056, PSB = 288, LSB = 144;
constexpr unsigned L_AUX = 131072;
constexpr unsigned L_QB = L_AUX, L_PL = L_QB + 16 * QSB, L_WM = L_PL + 16 * PSB, L_FS = L_WM + 512, L_KEY = L_FS + 1024, L_LS = L_KEY + 256, L_END = L_LS + 16 * LSB;
constexpr unsigned L_QP = L_AUX, L_LB = L_QP + 512, L_CB = L_LB + 16 * PSB, L_ST = L_CB + 8 * PSB;

__global__ __launch_bounds__(NT, 2) void k3_mix(const u16* __restrict__ x, const u16* __restrict__ c_kv,
                                               const u16* __restrict__ w_q, const u16* __restrict__ w_gate,
                                               const u16* __restrict__ w_up,
                                               const u16* __restrict__ w_uk, const u16* __restrict__ w_uv,
                                               const u16* __restrict__ w_o, const u16* __restrict__ w_down,
                                               const u16* __restrict__ gate_w, unsigned char* __restrict__ ws,
                                               u16* __restrict__ out) {
  __shared__ __attribute__((aligned(1024))) unsigned char sm[L_END];
  const unsigned sb = (unsigned)(size_t)sm;
  const int t = threadIdx.x, lane = t & 63, w = __builtin_amdgcn_readfirstlane(t >> 6);
  const int b = blockIdx.x;
  const int xcd = b & 7, idx = b >> 3;

  Tag T;
  {
    const u64 M48 = (1ull << 48) - 1;
    u64 u = (llvm_dispatch_id() & 0xffffffull) | ((((u64)__builtin_amdgcn_dispatch_ptr() >> 12) & 0xffffffull) << 24);
    u = (u + 0x5851f42d4c95ull) & M48;
    u ^= u >> 23; u = (u * 0x2545f4914f6dull) & M48;
    u ^= u >> 25; u = (u * 0x9e3779b97f4bull) & M48;
    u ^= u >> 24;
    T.t0 = (unsigned)u & 0xfffu; T.t1 = (unsigned)(u >> 12) & 0xfffu; T.t2 = (unsigned)(u >> 24) & 0xfffu; T.t3 = (unsigned)(u >> 36) & 0xfffu;
  }
  const int hq = xcd * 4 + (idx & 3), sq = idx >> 2;
  const bool roleT = idx < 16, wq4 = w < 4;
  unsigned fa[8], qxa = (sq * 64 + (lane >> 2)) * 2;
  fa[0] = ((lane >> (wq4 ? 2 : 4)) << (wq4 ? 13 : 16)) + (lane & (wq4 ? 3 : 15)) * 16 + hq * 256 + (wq4 ? sq * 524288 + w * 64 : (sq * 4 + w - 4) * 262144);
#pragma unroll
  for (int i = 1; i < 8; ++i) fa[i] = fa[0] + i * (wq4 ? 131072 : 8192);
  unsigned cpo = (idx & 3) == 0 ? (t & 255) * 128 : 0;
  int qo = (int)(WS_QP + ((hq * 8 + sq) * D + w * 32 + (lane & 3) * 8 + (lane >> 5) * 4) * 4);
  asm volatile("" : "+v"(fa[0]), "+v"(fa[1]), "+v"(fa[2]), "+v"(fa[3]), "+v"(fa[4]), "+v"(fa[5]), "+v"(fa[6]), "+v"(fa[7]), "+v"(qxa), "+v"(cpo), "+v"(qo));
  __builtin_amdgcn_sched_barrier(0);
  asm volatile("" :: "s"(x), "s"(c_kv), "s"(w_q), "s"(w_gate), "s"(w_up), "s"(w_uk), "s"(w_uv), "s"(w_o), "s"(w_down), "s"(gate_w), "s"(ws), "s"(out),
                     "s"(T.t0), "s"(T.t1), "s"(T.t2), "s"(T.t3));
  unsigned char* wsr = ws;
  asm volatile("" : "+s"(wsr));
  const __amdgpu_buffer_rsrc_t rsw = __builtin_amdgcn_make_buffer_rsrc((void*)wsr, 0, (int)WS_END, 0x00020000);
  u64 rt_start = __builtin_amdgcn_s_memrealtime();
  u16x4 vt[32];
  unsigned tra0, tra1;
  {
    const unsigned gq = lane >> 4, q = (lane & 15) >> 2, p = lane & 3;
    tra0 = sb + lds_off(8 * gq + q, 8 * w + (p >> 1)) + 8 * (p & 1);
    tra1 = sb + lds_off(8 * gq + 4 + q, 8 * w + (p >> 1)) + 8 * (p & 1);
  }
  auto trread = [&](int nt) {
    const unsigned ad0 = tra0 ^ (32u * nt), ad1 = tra1 ^ (32u * nt);
    const unsigned ad2 = ad0 + 65536u, ad3 = ad1 + 65536u;
    asm volatile("ds_read_b64_tr_b16 %0, %8\n\tds_read_b64_tr_b16 %1, %9\n\tds_read_b64_tr_b16 %2, %8 offset:32768\n\tds_read_b64_tr_b16 %3, %9 offset:32768\n\t"
                 "ds_read_b64_tr_b16 %4, %10\n\tds_read_b64_tr_b16 %5, %11\n\tds_read_b64_tr_b16 %6, %10 offset:32768\n\tds_read_b64_tr_b16 %7, %11 offset:32768"
                 : "=&v"(vt[nt * 8]), "=&v"(vt[nt * 8 + 1]), "=&v"(vt[nt * 8 + 2]), "=&v"(vt[nt * 8 + 3]), "=&v"(vt[nt * 8 + 4]), "=&v"(vt[nt * 8 + 5]), "=&v"(vt[nt * 8 + 6]), "=&v"(vt[nt * 8 + 7])
                 : "v"(ad0), "v"(ad1), "v"(ad2), "v"(ad3) : "memory");
  };
  auto vfrag = [&](int nt, int ks) {
    const v2u a0 = __builtin_bit_cast(v2u, vt[nt * 8 + 2 * ks]), a1 = __builtin_bit_cast(v2u, vt[nt * 8 + 2 * ks + 1]);
    return v4u{a0.x, a0.y, a1.x, a1.y};
  };
  auto pk = [](int c) { return (unsigned)(((c & 1) ? 4096 + swz(4) * 16 : 0) ^ ((c & 2) ? 8192 + swz(8) * 16 : 0)); };
  auto tile_rows = [&](const u16* src, int k, int j, int w) {
    const unsigned o = (lane * 16) ^ pk(j);
    glds16x4(src + w * 16 * 512, o ^ pk(k), o ^ (pk(k) ^ 32u), o ^ (pk(k) ^ 64u), o ^ (pk(k) ^ 96u), sb + w * 16384 + (k ^ j) * 4096);
  };

  unsigned hc[2];
  auto keys = [&](int first) {
    const unsigned kv = hkey(T, lane >= 32, lane & 31);
    hc[0] = (lane & first) == 0 ? hkey(T, false, 32) : 0u;
    hc[1] = (lane & first) == 0 ? hkey(T, true, 32) : 0u;
    asm volatile("ds_write_b32 %2, %3\n\ts_waitcnt lgkmcnt(0)" : "+v"(hc[0]), "+v"(hc[1]) : "v"(sb + L_KEY + lane * 4), "v"(kv) : "memory");
  };
  v4u wk[8];
  unsigned xr[4];
  auto q_partial = [&]() {
    v4u* wq = wk;
    const unsigned x01 = xr[0] | (xr[1] << 16), x23 = xr[2] | (xr[3] << 16);
    float acc[8], b4[4];
#pragma unroll
    for (int k = 0; k < 4; ++k) {
      acc[2 * k] = dot2(plo(wq[3][k], wq[2][k]), x23, dot2(plo(wq[1][k], wq[0][k]), x01, 0.f));
      acc[2 * k + 1] = dot2(phi(wq[3][k], wq[2][k]), x23, dot2(phi(wq[1][k], wq[0][k]), x01, 0.f));
    }
#pragma unroll
    for (int k = 0; k < 4; ++k) {
      float v = sw32(acc[k], acc[k + 4]);
      v += dppf<0x128>(v); v += dppf<0x124>(v);
      b4[k] = sw16(v, v);
    }
    if ((lane & 28) == 0) __builtin_amdgcn_raw_buffer_store_b128(enc(T, b4[0], b4[1], b4[2], b4[3]), rsw, qo, 0, 16);
  };

  if (__builtin_expect(wq4 || roleT, 1)) {
    u64 pc;
    unsigned len, keep;
    asm volatile("global_load_dwordx4 %0, %16, %25\n\tglobal_load_dwordx4 %1, %17, %25\n\tglobal_load_dwordx4 %2, %18, %25\n\tglobal_load_dwordx4 %3, %19, %25\n\t"
                 "s_mov_b32 %14, m0\n\ts_mov_b32 m0, %28\n\t"
                 "s_cmp_lt_u32 %27, 4\n\ts_cbranch_scc1 1f\n\t"
                 "global_load_dwordx4 %4, %20, %25\n\tglobal_load_dwordx4 %5, %21, %25\n\tglobal_load_dwordx4 %6, %22, %25\n\tglobal_load_dwordx4 %7, %23, %25\n\t"
                 "s_waitcnt vmcnt(0)\n\ts_branch 2f\n1:\n\t"
                 "global_load_ushort %8, %24, %26\n\tglobal_load_ushort %9, %24, %26 offset:32\n\tglobal_load_ushort %10, %24, %26 offset:64\n\tglobal_load_ushort %11, %24, %26 offset:96\n\t"
                 "s_getpc_b64 %12\n.Lcpf_here:\n\ts_mov_b32 %13, .Lcpf_end-.Lcpf_here+384\n\ts_cmp_lt_u32 %13, 0x10000\n\ts_cselect_b32 %13, %13, 0\n\t"
                 "v_min_u32 %15, %13, %15\n\tglobal_load_lds_dword %15, %12\n\t"
                 "s_waitcnt vmcnt(1)\n2:\n\ts_mov_b32 m0, %14"
                 : "=&v"(wk[0]), "=&v"(wk[1]), "=&v"(wk[2]), "=&v"(wk[3]), "=&v"(wk[4]), "=&v"(wk[5]), "=&v"(wk[6]), "=&v"(wk[7]),
                   "=&v"(xr[0]), "=&v"(xr[1]), "=&v"(xr[2]), "=&v"(xr[3]), "=&s"(pc), "=&s"(len), "=&s"(keep), "+v"(cpo)
                 : "v"(fa[0]), "v"(fa[1]), "v"(fa[2]), "v"(fa[3]), "v"(fa[4]), "v"(fa[5]), "v"(fa[6]), "v"(fa[7]), "v"(qxa),
                   "s"(wq4 ? w_q : w_uk), "s"(x), "s"(w), "s"(sb + L_FS) : "memory", "scc");
  } else {
    const u16* ct = c_kv + (size_t)(xcd * 8 + ((idx - 16) >> 1)) * KPR * DC;
    if (WDELAY) __builtin_amdgcn_s_sleep(WDELAY);
    tile_rows(ct, 0, 0, w);
  }
  asm volatile("" : "+s"(rt_start));
  if (__builtin_expect(wq4, 1)) {
    q_partial();
  }
  const unsigned t0 = (unsigned)rt_start;

  if (__builtin_expect(roleT, 1)) {
    const int h = xcd * 4 + (idx & 3), j = idx >> 2;
    const int src = xcd * 16 + idx;
    v2u ur[8];
    auto uv_load = [&]() {
      const unsigned c0 = j * 128 + ((w >> 1) & 1) * 64 + (w & 1) * 8 + (lane >> 4) * 2;
      const unsigned uo = (c0 * 4096 + h * 128 + (w >> 2) * 64 + (lane & 15) * 4) * 2;
#pragma unroll
      for (int i = 0; i < 8; ++i)
        asm volatile("global_load_dwordx2 %0, %1, %2" : "=v"(ur[i]) : "v"(uo), "s"(w_uv + ((i & 3) * 16 + (i >> 2)) * 4096) : "memory");
    };
    if (w < 4) {
      v4u qg = {0u, 0u, 0u, 0u}, qd;
      const unsigned oq = (unsigned)(WS_QP + ((h * 8 + (lane >> 3)) * D + w * 32 + (lane & 7) * 4) * 4);
      if (PRE_Q) __builtin_amdgcn_s_sleep(PRE_Q);
      u64 m = ~0ull;
      unsigned n = 0;
      for (;;) {
        poll1(qg, oq, ws, m);
        m = stale(chk(T, 0u, qg, qd));
        if (m == 0ull || ++n > SPIN_CAP) break;
        __builtin_amdgcn_s_sleep(SLP);
      }
      const f32x4 q4 = pay(qd);
      float qv = sw16(sw32(q4[0], q4[2]), sw32(q4[1], q4[3]));
      qv += dppf<0x128>(qv);
      *(float*)(sm + L_QP + (w * 32 + (lane & 7) * 4 + (lane >> 4)) * 4) = qv;
      WG_BARRIER();
    } else {
      int qso = (int)(WS_QA + (h * DC + j * 128 + (w - 4) * 32 + (lane >> 4) * 8 + ((lane >> 3) & 1) * 4) * 4);
      asm volatile("" : "+v"(qso));
      f32x2 wf[32];
#pragma unroll
      for (int i = 0; i < 8; ++i)
#pragma unroll
        for (int k = 0; k < 4; ++k) wf[4 * i + k] = f32x2{bf_lo(wk[i][k]), bf_hi(wk[i][k])};
#pragma unroll
      for (int i = 0; i < 32; i += 8)
        asm volatile("" : "+v"(wf[i]), "+v"(wf[i + 1]), "+v"(wf[i + 2]), "+v"(wf[i + 3]), "+v"(wf[i + 4]), "+v"(wf[i + 5]), "+v"(wf[i + 6]), "+v"(wf[i + 7]));
      __builtin_amdgcn_s_barrier();
      asm volatile("" ::: "memory");
      f32x4 q0, q1;
      asm volatile("ds_read_b128 %0, %2\n\tds_read_b128 %1, %2 offset:16\n\ts_waitcnt lgkmcnt(0)" : "=&v"(q0), "=&v"(q1) : "v"(sb + L_QP + (lane & 15) * 32) : "memory");
      const float scale = 0.08838834764831845f * 1.4426950408889634f;
      const f32x2 qa = {q0[0], q0[1]}, qb = {q0[2], q0[3]}, qc = {q1[0], q1[1]}, qd = {q1[2], q1[3]};
      float v[8], r4[4];
#pragma unroll
      for (int i = 0; i < 8; ++i) {
        const f32x2 a = (qa * wf[4 * i] + qb * wf[4 * i + 1]) + (qc * wf[4 * i + 2] + qd * wf[4 * i + 3]);
        v[i] = a[0] + a[1];
      }
#pragma unroll
      for (int i = 0; i < 4; ++i) {
        float e;
        asm("s_nop 1\n\tv_add_f32_dpp %0, %1, %1 row_ror:8 row_mask:0xf bank_mask:0x3\n\tv_add_f32_dpp %0, %2, %2 row_ror:8 row_mask:0xf bank_mask:0xc\n\ts_nop 1"
            : "=&v"(e) : "v"(v[i]), "v"(v[i + 4]));
        e += dppf<0xB1>(e); e += dppf<0x4E>(e); e += dppf<0x141>(e);
        r4[i] = e * scale;
      }
      const unsigned p01 = pack2(r4[0], r4[1]), p23 = pack2(r4[2], r4[3]);
      if ((lane & 7) == 0) __builtin_amdgcn_raw_buffer_store_b128(v4u{(p01 << 16) | T.t0, (p01 & 0xffff0000u) | T.t1, (p23 << 16) | T.t2, (p23 & 0xffff0000u) | T.t3}, rsw, qso, 0, 16);
    }
    __builtin_amdgcn_s_barrier();
    uv_load();
    v4u hk[2];
    keys(7);
    asm volatile("ds_read_b128 %0, %2\n\tds_read_b128 %1, %2 offset:128\n\ts_waitcnt lgkmcnt(0)" : "=&v"(hk[0]), "=&v"(hk[1]) : "v"(sb + L_KEY + (lane & 7) * 16) : "memory");
    if (TSD) __builtin_amdgcn_s_sleep(TSD);
    v4u xa = aload(x, lane * 16), ga = aload(gate_w, lane * 16);
#pragma unroll
    for (int k = 0; k < 4; ++k) tile_rows(w_o + (size_t)h * D * MD, k, j, (w + 2 * j) & 7);
    asm volatile("s_waitcnt vmcnt(16)" : "+v"(xa), "+v"(ga), "+v"(ur[0]), "+v"(ur[1]), "+v"(ur[2]), "+v"(ur[3]), "+v"(ur[4]), "+v"(ur[5]), "+v"(ur[6]), "+v"(ur[7]) :: "memory");
    const float gatev = gate_of(xa, ga);
    v4u ub[4];
#pragma unroll
    for (int k = 0; k < 4; ++k) {
      ub[0][k] = plo(ur[2 * k + 1].x, ur[2 * k].x); ub[1][k] = phi(ur[2 * k + 1].x, ur[2 * k].x);
      ub[2][k] = plo(ur[2 * k + 1].y, ur[2 * k].y); ub[3][k] = phi(ur[2 * k + 1].y, ur[2 * k].y);
    }
    asm volatile("" : "+v"(ub[0]), "+v"(ub[1]), "+v"(ub[2]), "+v"(ub[3]));
    const unsigned unit = h * 4 + j;
    unsigned po = (unsigned)(WS_PART + (unit * NR + w * 8) * 256 + lane * 16);
    unsigned mo = (unsigned)(WS_CK + ((((w * 8 + ((lane >> 2) & 1) * 4 + (lane >> 4)) * 2 + (h >> 4)) * 8 + 2 * j + ((lane >> 3) & 1)) * 16 + (h & 15)) * 16);
    unsigned a_lw = sb + L_LB + (2 * w) * PSB + (lane & 15) * 16 + (lane >> 4) * 4;
    unsigned a_ew = sb + L_ST + (w * 8 + ((lane >> 2) & 1) * 4 + (lane >> 4)) * 4;
    unsigned a_lr = sb + L_LB + (lane & 15) * PSB + (w & 3) * 64 + (lane >> 4) * 16;
    unsigned a_er = sb + L_ST + lane * 4;
    unsigned a_cw = sb + L_CB + (w & 3) * 2 * PSB + ((w >> 2) * 64 + (lane & 15) * 4 + (lane >> 4)) * 2;
    unsigned a_cr = sb + L_CB + (lane & 7) * PSB + (lane >> 4) * 16;
    unsigned a_fl = sb + L_ST + 256;
    asm volatile("ds_write_b32 %0, %1" : "+v"(a_fl) : "v"(0u) : "memory");
    unsigned xo = (unsigned)(WS_M + (h * NR + lane) * 16);
    unsigned a_bp = (w * 8 + (lane >> 4)) * 4, a_bs = a_ew - (sb + L_ST);
    v4u em;
#pragma unroll
    for (int k = 0; k < 4; ++k) em[k] = (lane & 14) == 4 * k ? ((lane & 1) ? 0xffff0000u : 0xffffu) : 0u;
    int o_fin = (lane & 3) == 0 ? (int)(WS_FIN + src * 2048 + (w * 16 + (lane & 12) + (lane >> 4)) * 16) : (int)0x7ffffff0;
    unsigned mk = 0xfffff000u;
    float gv = gatev, a[4] = {0.f, 0.f, 0.f, 0.f};
    asm volatile("" : "+v"(a[0]), "+v"(a[1]), "+v"(a[2]), "+v"(a[3]));
    asm volatile("" : "+v"(po), "+v"(mo), "+v"(a_lw), "+v"(a_ew), "+v"(a_lr), "+v"(a_er), "+v"(a_cw), "+v"(a_cr), "+v"(o_fin), "+v"(mk), "+v"(gv),
                      "+v"(xo), "+v"(a_bp), "+v"(a_bs), "+v"(em));
    VMWAIT(0);
    __builtin_amdgcn_s_barrier();
#pragma unroll
    for (int nt = 0; nt < 4; ++nt) trread(nt);
    unsigned tb;
    {
      v4u an = {0u, 0u, 0u, 0u}, ad;
      const unsigned ao = (unsigned)(WS_ANN + ((h >> 4) * NR + w * 8 + (lane & 7)) * 128);
      unsigned n = 0;
      for (;;) {
        poll1(an, ao, ws, ~0ull);
        if (stale(chk(T, 0u, an, ad)) == 0ull || ++n > SPIN_CAP) break;
        __builtin_amdgcn_s_sleep(2);
      }
      const unsigned tn = now();
      int dlt = (int)(an.x - (tn << 12)) >> 12;
      dlt = max(dlt, (int)dppu<0xB1>((unsigned)dlt)); dlt = max(dlt, (int)dppu<0x4E>((unsigned)dlt)); dlt = max(dlt, (int)dppu<0x141>((unsigned)dlt));
      tb = tn + __builtin_amdgcn_readfirstlane(dlt) + DB;
    }
    v4u pb[2], ck, ae[2];
    float es;
#define LD3W(W) "global_load_dwordx4 %2, %4, " W " sc1\n\tglobal_load_dwordx4 %0, %3, " W " sc1\n\tglobal_load_dwordx4 %1, %3, " W " offset:1024 sc1"
#define LD3 LD3W("%5")
    {
      v4u mg, md;
      wait_until(tb - DM);
      asm volatile("global_load_dwordx4 %0, %1, %2 sc1" : "=v"(mg) : "v"(xo), "s"(ws) : "memory");
      wait_until(tb);
      asm volatile(LD3 "\n\ts_waitcnt vmcnt(3)" : "=&v"(pb[0]), "=&v"(pb[1]), "=&{v[252:255]}"(ck), "+v"(po), "+v"(mo) : "s"(ws) : "memory");
      asm volatile("" : "+v"(mg));
      u64 m = stale(chk(T, 0u, mg, md));
      if (__builtin_expect(m != 0ull, 0)) {
        unsigned n = 0;
        do {
          poll1(mg, xo, ws, m);
          m = stale(chk(T, 0u, mg, md));
        } while (m != 0ull && ++n <= SPIN_CAP);
        asm volatile(LD3 : "+v"(pb[0]), "+v"(pb[1]), "+{v[252:255]}"(ck), "+v"(po), "+v"(mo) : "s"(ws) : "memory");
      }
      const float mr = asf(md.x | ((md.y >> 12) & 0xfffu));
      float M = vmax(mr, dppf<0x128>(mr)); M = vmax(M, dppf<0x124>(M)); M = vmax(M, dppf<0x122>(M)); M = vmax(M, dppf<0x121>(M));
      M = xrow_vmax(M);
      const float e = __builtin_amdgcn_exp2f(mr - M);
      const unsigned eb = pack2(e, e);
      unsigned e0, e1, e2;
      asm volatile("ds_bpermute_b32 %0, %3, %5\n\tds_bpermute_b32 %1, %3, %5 offset:16\n\tds_bpermute_b32 %2, %4, %5\n\ts_waitcnt lgkmcnt(0)"
                   : "=&v"(e0), "=&v"(e1), "=&v"(e2) : "v"(a_bp), "v"(a_bs), "v"(eb) : "memory");
      ae[0] = em & e0; ae[1] = em & e1;
      es = bf_hi(e2);
      asm volatile("" : "+v"(ae[0]), "+v"(ae[1]), "+v"(es));
    }
    const unsigned tg = T.t0 | (T.t1 << 12) | (T.t2 << 24);
#define MF "v_mfma_f32_16x16x32_bf16 "
#define UD(o, a, k) "v_dot2_u32_u16 %" #o ", %" #a ", %" #k ", %" #o "\n\t"
#define UD0(o, a, k, c) "v_dot2_u32_u16 %" #o ", %" #a ", %" #k ", %" #c "\n\t"
    for (unsigned att = 1;; ++att) {
      unsigned hs[2][2];
      {
        f32x4 lm;
        float el;
        unsigned n = 0;
        u64 m1, m2;
        asm volatile("s_branch 1f\n"
                     "2:\n\ts_lshr_b64 %8, vcc, 4\n\ts_lshl_b64 %9, vcc, 4\n\ts_and_b64 %8, %8, %16\n\ts_andn2_b64 %9, %9, %16\n\ts_or_b64 %8, %8, %9\n\ts_or_b64 %8, %8, vcc\n\t"
                     "s_add_u32 %7, %7, 1\n\ts_cmp_gt_u32 %7, %17\n\ts_cbranch_scc1 3f\n\ts_sleep %18\n\ts_waitcnt vmcnt(0)\n\t"
                     "s_mov_b64 exec, %8\n\t" LD3W("%19") "\n\ts_mov_b64 exec, -1\n"
                     "1:\n\ts_waitcnt vmcnt(2)\n\tv_cmp_ne_u32 vcc, %11, v255\n\tv_mul_f32 %10, %12, v254\n\ts_cbranch_vccnz 2b\n"
                     "3:\n\ts_waitcnt vmcnt(1)\n\t" MF "%6, %13, %0, 0\n\tds_write_b32 %5, %10\n\ts_waitcnt vmcnt(0)\n\t" MF "%6, %14, %1, %6"
                     : "+v"(pb[0]), "+v"(pb[1]), "+{v[252:255]}"(ck), "+v"(po), "+v"(mo), "+v"(a_ew), "=&v"(lm), "+s"(n), "=&s"(m1), "=&s"(m2), "=&v"(el)
                     : "s"(tg), "v"(es), "v"(ae[0]), "v"(ae[1]), "n"(0), "s"(0x0f0f0f0f0f0f0f0full), "n"(SPIN_CAP), "n"(SLP), "s"(ws) : "memory", "vcc", "scc");
        asm volatile(UD0(0, 4, 8, 12) UD0(1, 5, 8, 12) UD0(2, 4, 10, 13) UD0(3, 5, 10, 13) UD(0, 6, 9) UD(1, 7, 9) UD(2, 6, 11) UD(3, 7, 11)
                     : "=&v"(hs[0][0]), "=&v"(hs[0][1]), "=&v"(hs[1][0]), "=&v"(hs[1][1])
                     : "v"(pb[0].x), "v"(pb[1].x), "v"(pb[0].y), "v"(pb[1].y), "v"(hk[0][0]), "v"(hk[0][1]), "v"(hk[1][0]), "v"(hk[1][1]), "v"(hc[0]), "v"(hc[1]));
        asm volatile("" : "+v"(lm));
        const unsigned hi = pack2(lm[0], lm[1]), lo = pack2(lm[0] - bf_lo(hi), lm[1] - bf_hi(hi));
        asm volatile("ds_write2_b32 %0, %1, %2 offset1:72" :: "v"(a_lw), "v"(hi), "v"(lo) : "memory");
      }
      WG_BARRIER();
      float ls;
      {
        v4u la;
        unsigned f0, f1, x0;
        u64 c0;
        f32x4 dd[4];
#define UHM(o, i, bm) "v_add_u32_dpp %" #o ", %" #i ", %" #i " row_half_mirror row_mask:0xf bank_mask:" #bm "\n\t"
#define UQA(o, qp) "v_add_u32_dpp %" #o ", %" #o ", %" #o " quad_perm:" qp " row_mask:0xf bank_mask:0xf\n\t"
        asm volatile("ds_read_b128 %4, %6\n\tds_read_b32 %5, %7\n\t"
                     UD(0, 8, 12) UD(1, 9, 12) UD(2, 8, 14) UD(3, 9, 14) UD(0, 10, 13) UD(1, 11, 13) UD(2, 10, 15) UD(3, 11, 15)
                     : "+v"(hs[0][0]), "+v"(hs[0][1]), "+v"(hs[1][0]), "+v"(hs[1][1]), "=&v"(la), "=&v"(ls)
                     : "v"(a_lr), "v"(a_er), "v"(pb[0].z), "v"(pb[1].z), "v"(pb[0].w), "v"(pb[1].w), "v"(hk[0][2]), "v"(hk[0][3]), "v"(hk[1][2]), "v"(hk[1][3]) : "memory");
        asm volatile(UHM(0, 8, 0x5) UHM(0, 9, 0xa) "v_sub_u32 %7, v252, v254\n\t" UHM(1, 10, 0x5) UHM(1, 11, 0xa) UQA(0, "[1,0,3,2]")
                     "s_waitcnt lgkmcnt(1)\n\t"
                     MF "%2, %12, %13, 0\n\t" UQA(1, "[1,0,3,2]") UQA(0, "[2,3,0,1]") MF "%3, %12, %14, 0\n\t" UQA(1, "[2,3,0,1]") "v_cmp_ne_u32 %6, %0, %7\n\t"
                     MF "%4, %12, %15, 0\n\tv_cmp_ne_u32 vcc, %1, v253\n\t" MF "%5, %12, %16, 0\n\t"
                     "s_or_b64 exec, %6, vcc\n\tds_write_b32 %17, %18\n\ts_mov_b64 exec, -1"
                     : "=&v"(f0), "=&v"(f1), "=&v"(dd[0]), "=&v"(dd[1]), "=&v"(dd[2]), "=&v"(dd[3]), "=&s"(c0), "=&v"(x0)
                     : "v"(hs[0][0]), "v"(hs[0][1]), "v"(hs[1][0]), "v"(hs[1][1]), "v"(la), "v"(ub[0]), "v"(ub[1]), "v"(ub[2]), "v"(ub[3]), "v"(a_fl), "v"(att), "{v[252:255]}"(ck)
                     : "memory", "vcc", "scc");
        float c4[4];
#pragma unroll
        for (int jj = 0; jj < 4; ++jj) {
          asm volatile("" : "+v"(dd[jj]));
          const f32x2 u = f32x2{dd[jj][0], dd[jj][1]} + f32x2{dd[jj][2], dd[jj][3]};
          c4[jj] = u[0] + u[1];
          asm volatile("" : "+v"(c4[jj]));
        }
        const float cv = sw16(sw32(c4[0], c4[2]), sw32(c4[1], c4[3]));
        const unsigned hi = pack2(cv, cv), lo = pack2(cv - bf_lo(hi), 0.f);
        asm volatile("ds_write_b16 %0, %1\n\tds_write_b16 %0, %2 offset:288" :: "v"(a_cw), "v"(hi), "v"(lo) : "memory");
      }
      WG_BARRIER();
      v4u pf[4];
      unsigned fl;
      asm volatile("ds_read_b128 %0, %5\n\tds_read_b128 %1, %5 offset:64\n\tds_read_b128 %2, %5 offset:128\n\tds_read_b128 %3, %5 offset:192\n\tds_read_b32 %4, %6\n\ts_waitcnt lgkmcnt(1)"
                   : "=&v"(pf[0]), "=&v"(pf[1]), "=&v"(pf[2]), "=&v"(pf[3]), "=&v"(fl) : "v"(a_cr), "v"(a_fl) : "memory");
      f32x4 acc[4];
      auto chain = [&](int nt, int k0, int k1) {
#pragma unroll
        for (int ks = k0; ks < k1; ++ks) acc[nt] = mfma(vfrag(nt, ks), pf[ks], ks ? acc[nt] : f32x4{0.f, 0.f, 0.f, 0.f});
        __builtin_amdgcn_sched_barrier(0);
      };
#define HM(o, i, bm) "v_add_f32_dpp %" #o ", %" #i ", %" #i " row_half_mirror row_mask:0xf bank_mask:" #bm "\n\t"
#define HM2(pre, c, bm, nt, k) do { asm volatile(pre HM(0, 2, bm) HM(1, 3, bm) : c(a[k]), c(a[k + 1]) : "v"(acc[nt][k]), "v"(acc[nt][k + 1])); __builtin_amdgcn_sched_barrier(0); } while (0)
      __builtin_amdgcn_sched_barrier(0);
      asm volatile("" : "+v"(ls));
      chain(0, 0, 1); ls += dppf<0x128>(ls); asm volatile("" : "+v"(ls)); SB;
      chain(0, 1, 3); ls += dppf<0x124>(ls); asm volatile("" : "+v"(ls)); SB;
      chain(0, 3, 4); chain(1, 0, 1); ls += dppf<0x122>(ls); asm volatile("" : "+v"(ls)); SB;
      chain(1, 1, 3); ls += dppf<0x121>(ls); asm volatile("" : "+v"(ls)); SB;
      chain(1, 3, 4); ls = sw16(ls, ls); asm volatile("" : "+v"(ls)); SB;
      u64 redo;
      int o_st;
      asm volatile("s_waitcnt lgkmcnt(0)\n\tv_cmp_eq_u32 %0, %2, %3\n\ts_nop 1\n\tv_cndmask_b32 %1, %4, %5, %0" : "=&s"(redo), "=v"(o_st) : "v"(fl), "v"(att), "v"(o_fin), "v"(att > SPIN_CAP ? o_fin : (int)0x7ffffff0) : "memory");
      __builtin_amdgcn_sched_barrier(0);
      chain(2, 0, 2); ls = sw32(ls, ls); asm volatile("" : "+v"(ls)); SB;
      chain(2, 2, 3);
      float sc = gv * __builtin_amdgcn_rcpf(ls);
      asm volatile("" : "+v"(sc)); SB;
      chain(2, 3, 4);
      chain(3, 0, 1); HM2("", "+v", 0x1, 0, 0);
      chain(3, 1, 2); HM2("", "+v", 0x1, 0, 2);
      chain(3, 2, 3); HM2("", "+v", 0x2, 1, 0);
      chain(3, 3, 4); HM2("", "+v", 0x2, 1, 2);
      HM2("", "+v", 0x4, 2, 0); HM2("", "+v", 0x4, 2, 2);
      HM2("s_nop 1\n\t", "+v", 0x8, 3, 0); HM2("", "+v", 0x8, 3, 2);
#define QA(o, qp) "v_add_f32_dpp %" #o ", %" #o ", %" #o " quad_perm:" qp " row_mask:0xf bank_mask:0xf\n\t"
      asm volatile(QA(0, "[1,0,3,2]") QA(1, "[1,0,3,2]") QA(2, "[1,0,3,2]") QA(3, "[1,0,3,2]") QA(0, "[2,3,0,1]") QA(1, "[2,3,0,1]") QA(2, "[2,3,0,1]") QA(3, "[2,3,0,1]")
                   : "+v"(a[0]), "+v"(a[1]), "+v"(a[2]), "+v"(a[3]));
      __builtin_amdgcn_raw_buffer_store_b128(encm(T, mk, f32x4{a[0] * sc, a[1] * sc, a[2] * sc, a[3] * sc}), rsw, o_st, 0, 16);
      if (__builtin_expect(redo == 0ull || att > SPIN_CAP, 1)) { break; }
      asm volatile("s_waitcnt vmcnt(0)\n\t" LD3 : "+v"(pb[0]), "+v"(pb[1]), "+{v[252:255]}"(ck), "+v"(po), "+v"(mo) : "s"(ws) : "memory");
    }
    asm volatile("s_endpgm" ::: "memory");
  } else {
    const int ai = idx - 16;
    const int r = xcd * 8 + (ai >> 1), g = ai & 1;
    const int fs = ((ai >> 2) * 8 + xcd) * 4 + (ai & 3);
    const bool has_ffn = fs < NFS;
    const u16* ct = c_kv + (size_t)r * KPR * DC;
    __builtin_amdgcn_s_barrier();
#pragma unroll
    for (int k = 0; k < 4; ++k)
      if (w < 4 || k != 3 * g) tile_rows(ct, k, 3 * g, w);
    v4u afr[16];
    v4u ag[4];
    const int fu = (xcd * 16 + ai) * 6 + w;
    const bool ffn_p = w < 6 && fu < 688;
    const int fk = ffn_p ? fu / 43 : 0, fg = ffn_p ? fu - fk * 43 : 0;
    unsigned a_fx = (fk * 32 + (lane >> 2)) * 2;
    unsigned a_fg = ((fk * 32 + (lane >> 2)) * FF + fg * 32 + (lane & 3) * 8) * 2;
    unsigned qo = (unsigned)(WS_QA + (g * 16 + 2 * w) * 2048 + lane * 16);
    unsigned a_qw = sb + L_QB + 2 * w * QSB + lane * 8;
    unsigned a_b = sb + L_QB + (lane & 15) * QSB + (lane >> 4) * 16;
    unsigned a_mw = sb + L_WM + ((lane & 15) * 8 + w) * 4;
    unsigned a_mr = sb + L_WM + (lane & 15) * 32;
    unsigned a_pw = sb + L_PL + (lane & 15) * PSB + w * 32 + (lane >> 4) * 8;
    unsigned a_pr = sb + L_PL + (lane & 15) * PSB + (lane >> 4) * 16;
    int o_pv = (int)(WS_PART + (((g * 16 + (lane >> 4) * 4) * 4 + (w >> 1)) * NR + r) * 256 + (w & 1) * 128 + (lane & 15) * 8);
    int o_ml = lane < 8 ? (int)(WS_ANN + (g * NR + r) * 128 + lane * 16) : (int)0x7ffffff0;
    int o_ck = (lane & 3) == 0 ? (int)(WS_CK + ((r * 2 + g) * 8 + w) * 256 + (lane >> 2) * 16) : (int)0x7ffffff0;
    int o_m = lane < 16 ? (int)(WS_M + ((g * 16 + lane) * NR + r) * 16) : (int)0x7ffffff0;
    v4u mgr = {0u, 0u, T.t2, T.t3};
    unsigned tg = T.t0 | (T.t1 << 12) | (T.t2 << 24);
    int so1 = 4 * NR * 256, so2 = 2 * so1, so3 = 3 * so1;
    asm volatile("" : "+v"(mgr), "+v"(tg), "+s"(so1), "+s"(so2), "+s"(so3));
    unsigned a_sw = sb + L_LS + (lane & 15) * LSB + (w * 4 + (lane >> 4)) * 4;
    unsigned a_sr = sb + L_LS + (lane >> 2) * LSB + (lane & 3) * 32;
    asm volatile("" : "+v"(a_sw), "+v"(a_sr));
    v2u hk[2];
    keys(15);
    asm volatile("ds_read_b64 %0, %4\n\tds_read_b64 %1, %4 offset:128\n\ts_waitcnt lgkmcnt(0)" : "=&v"(hk[0]), "=&v"(hk[1]), "+v"(o_ck), "+v"(o_m) : "v"(sb + L_KEY + (lane & 15) * 8) : "memory");
    int o_fp = ffn_p && (lane & 12) == 0 ? (int)(WS_FP + (fk * 688 + fg * 16 + (lane & 3) * 4 + (lane >> 4)) * 16) : (int)0x7ffffff0;
    asm volatile("" : "+v"(o_fp));
    unsigned mk = 0xfffff000u;
    f32x4 sacc = {0.f, 0.f, 0.f, 0.f};
    unsigned one = 0x3f803f80u;
    unsigned a_a[4];
#pragma unroll
    for (int i = 0; i < 4; ++i) a_a[i] = sb + lds_off(w * 16 + (lane & 15), 4 * i + (lane >> 4));
    asm volatile("" : "+v"(a_a[0]), "+v"(a_a[1]), "+v"(a_a[2]), "+v"(a_a[3]));
    asm volatile("" : "+v"(a_fx), "+v"(a_fg), "+v"(qo), "+v"(a_qw), "+v"(a_b), "+v"(a_mw), "+v"(a_mr), "+v"(a_pw), "+v"(a_pr), "+v"(o_pv), "+v"(o_ml), "+v"(mk), "+v"(tra0), "+v"(tra1), "+v"(sacc), "+v"(one));
    VMWAIT(PAW);
    if (TA) wait_until(t0 + TA);
    issue4(ag, qo, ws);
    VMWAIT(4);
    v4u wg[2], wu[2];
    unsigned xf[2];
    auto ffn_load = [&]() {
      asm volatile("global_load_dwordx4 %0, %6, %7\n\tglobal_load_dwordx4 %1, %6, %8\n\tglobal_load_dwordx4 %2, %6, %9\n\tglobal_load_dwordx4 %3, %6, %10\n\t"
                   "global_load_ushort %4, %11, %12\n\tglobal_load_ushort %5, %11, %12 offset:32"
                   : "=&v"(wg[0]), "=&v"(wu[0]), "=&v"(wg[1]), "=&v"(wu[1]), "=&v"(xf[0]), "=&v"(xf[1])
                   : "v"(a_fg), "s"(w_gate), "s"(w_up), "s"(w_gate + 16 * FF), "s"(w_up + 16 * FF), "v"(a_fx), "s"(x) : "memory");
    };
    auto ffn_part = [&]() {
      const unsigned xp = xf[0] | (xf[1] << 16);
      float acc[16];
#pragma unroll
      for (int k = 0; k < 4; ++k) {
        acc[4 * k] = dot2(plo(wg[1][k], wg[0][k]), xp, 0.f);
        acc[4 * k + 1] = dot2(plo(wu[1][k], wu[0][k]), xp, 0.f);
        acc[4 * k + 2] = dot2(phi(wg[1][k], wg[0][k]), xp, 0.f);
        acc[4 * k + 3] = dot2(phi(wu[1][k], wu[0][k]), xp, 0.f);
      }
      float h4[4];
#pragma unroll
      for (int k = 0; k < 4; ++k) {
        float v = sw16(sw32(acc[k], acc[k + 8]), sw32(acc[k + 4], acc[k + 12]));
        v += dppf<0x128>(v); v += dppf<0x124>(v);
        h4[k] = v;
      }
      __builtin_amdgcn_raw_buffer_store_b128(encm(T, mk, f32x4{h4[0], h4[1], h4[2], h4[3]}), rsw, o_fp, 0, 16);
    };
#pragma unroll
    for (int i = 0; i < 16; i += 4)
      asm volatile("ds_read_b128 %0, %4 offset:%8\n\tds_read_b128 %1, %5 offset:%8\n\tds_read_b128 %2, %6 offset:%8\n\tds_read_b128 %3, %7 offset:%8"
                   : "=&v"(afr[i]), "=&v"(afr[i + 1]), "=&v"(afr[i + 2]), "=&v"(afr[i + 3]) : "v"(a_a[0]), "v"(a_a[1]), "v"(a_a[2]), "v"(a_a[3]), "n"(64 * i) : "memory");
    {
      auto qput = [&]() {
        asm volatile("ds_write2st64_b64 %0, %1, %2 offset1:1\n\tds_write2_b64 %0, %3, %4 offset0:132 offset1:196"
                     :: "v"(a_qw), "v"(v2u{phi(ag[0].y, ag[0].x), phi(ag[0].w, ag[0].z)}), "v"(v2u{phi(ag[1].y, ag[1].x), phi(ag[1].w, ag[1].z)}),
                        "v"(v2u{phi(ag[2].y, ag[2].x), phi(ag[2].w, ag[2].z)}), "v"(v2u{phi(ag[3].y, ag[3].x), phi(ag[3].w, ag[3].z)}) : "memory");
        unsigned e[8];
#pragma unroll
        for (int i = 0; i < 2; ++i) {
          e[4 * i] = (ag[2 * i].x ^ T.t0) | (ag[2 * i + 1].x ^ T.t0); e[4 * i + 1] = (ag[2 * i].y ^ T.t1) | (ag[2 * i + 1].y ^ T.t1);
          e[4 * i + 2] = (ag[2 * i].z ^ T.t2) | (ag[2 * i + 1].z ^ T.t2); e[4 * i + 3] = (ag[2 * i].w ^ T.t3) | (ag[2 * i + 1].w ^ T.t3);
        }
        asm volatile("" : "+v"(e[0]), "+v"(e[1]), "+v"(e[2]), "+v"(e[3]), "+v"(e[4]), "+v"(e[5]), "+v"(e[6]), "+v"(e[7]));
        const unsigned bad = (e[0] | e[1] | e[2]) | (e[3] | e[4] | e[5]) | (e[6] | e[7]);
        u64 m;
        asm volatile("v_cmp_ne_u16 %0, 0, %1" : "=s"(m) : "v"(bad));
        return m;
      };
      vw4<0>(ag);
      unsigned n = 0;
      for (;;) {
        const u64 m = qput();
        if (m == 0ull || ++n > SPIN_CAP) break;
        __builtin_amdgcn_s_sleep(SLP);
        poll4(ag, qo, ws, m);
      }
    }
    WG_BARRIER();
    {
      v4u bfr[16];
#define BRD(q) asm volatile("ds_read_b128 %0, %4 offset:%5\n\tds_read_b128 %1, %4 offset:%6\n\tds_read_b128 %2, %4 offset:%7\n\tds_read_b128 %3, %4 offset:%8" \
                   : "=&v"(bfr[4 * (q)]), "=&v"(bfr[4 * (q) + 1]), "=&v"(bfr[4 * (q) + 2]), "=&v"(bfr[4 * (q) + 3]) \
                   : "v"(a_b), "n"(256 * (q)), "n"(256 * (q) + 64), "n"(256 * (q) + 128), "n"(256 * (q) + 192) : "memory")
      BRD(0); BRD(1);
      if (ffn_p) ffn_load();
#define SC4(q, cnt, c0, post) asm volatile("s_waitcnt lgkmcnt(" #cnt ")\n\t" MF "%0, %1, %5, " c0 "\n\t" MF "%0, %2, %6, %0\n\t" MF "%0, %3, %7, %0\n\t" MF "%0, %4, %8, %0" post \
                   : "+v"(sacc) : "v"(afr[4 * (q)]), "v"(afr[4 * (q) + 1]), "v"(afr[4 * (q) + 2]), "v"(afr[4 * (q) + 3]), \
                     "v"(bfr[4 * (q)]), "v"(bfr[4 * (q) + 1]), "v"(bfr[4 * (q) + 2]), "v"(bfr[4 * (q) + 3]) : "memory")
      if (w == 7) { const unsigned tn = now(); __builtin_amdgcn_raw_buffer_store_b128(v4u{(tn << 12) | T.t0, T.t1, T.t2, T.t3}, rsw, o_ml, 0, 16); }
      BRD(2); BRD(3);
      SC4(0, 12, "0", "");
      SC4(1, 8, "%0", "");
      SC4(2, 4, "%0", "");
      asm volatile("s_waitcnt lgkmcnt(0)" ::: "memory");
      trread(0); trread(1);
      SC4(3, 15, "%0", "\n\ts_nop 7");
    }
    {
      const float mx = xrow_vmax(vmax3(vmax(sacc[0], sacc[3]), sacc[1], sacc[2]));
      asm volatile("ds_write_b32 %0, %1" :: "v"(a_mw), "v"(mx) : "memory");
    }
    WG_BARRIER();
    float mref;
    {
      f32x4 m0, m1;
      asm volatile("ds_read_b128 %0, %2\n\tds_read_b128 %1, %2 offset:16" : "=&v"(m0), "=&v"(m1) : "v"(a_mr) : "memory");
      trread(2);
      asm volatile("s_waitcnt lgkmcnt(8)" : "+v"(m0), "+v"(m1) :: "memory");
      mref = __builtin_ceilf(vmax3(vmax3(m0[0], m0[1], m0[2]), vmax3(m0[3], m1[0], m1[1]), vmax(m1[2], m1[3])));
      const v2u pp = {pack2(__builtin_amdgcn_exp2f(sacc[0] - mref), __builtin_amdgcn_exp2f(sacc[1] - mref)),
                      pack2(__builtin_amdgcn_exp2f(sacc[2] - mref), __builtin_amdgcn_exp2f(sacc[3] - mref))};
      float lp;
      asm volatile("v_dot2_f32_bf16 %0, %4, %6, 0\n\tds_write_b64 %1, %3\n\tv_dot2_f32_bf16 %0, %5, %6, %0\n\ts_nop 2\n\tds_write_b32 %2, %0"
                   : "=&v"(lp) : "v"(a_pw), "v"(a_sw), "v"(pp), "v"(pp.x), "v"(pp.y), "v"(one) : "memory");
    }
    trread(3);
    if (w == 6) {
      mgr.x = (asu(mref) & mk) | T.t0; mgr.y = (asu(mref) << 12) | T.t1;
      __builtin_amdgcn_raw_buffer_store_b128(mgr, rsw, o_m, 0, 16);
    }
    asm volatile("s_waitcnt lgkmcnt(8)" ::: "memory");
    __builtin_amdgcn_s_barrier();
    asm volatile("" ::: "memory");
    {
      v4u pf[4];
      f32x4 s0, s1;
      if (w < 4) __builtin_amdgcn_s_setprio(3);
      asm volatile("ds_read_b128 %0, %6\n\tds_read_b128 %1, %6 offset:64\n\tds_read_b128 %2, %6 offset:128\n\tds_read_b128 %3, %6 offset:192\n\t"
                   "ds_read_b128 %4, %7\n\tds_read_b128 %5, %7 offset:16"
                   : "=&v"(pf[0]), "=&v"(pf[1]), "=&v"(pf[2]), "=&v"(pf[3]), "=&v"(s0), "=&v"(s1) : "v"(a_pr), "v"(a_sr) : "memory");
      f32x4 acc[4];
#define PV2(pre, c, c0, c1, nt, ks) asm volatile(pre MF "%0, %2, %3, " c0 "\n\t" MF "%1, %2, %4, " c1 : c(acc[nt]), c(acc[nt + 1]) : "v"(pf[ks]), "v"(vfrag(nt, ks)), "v"(vfrag(nt + 1, ks)) : "memory")
#define PV1(nt, ks) asm volatile(MF "%0, %1, %2, %0" : "+v"(acc[nt]) : "v"(pf[ks]), "v"(vfrag(nt, ks)))
      v2u pw[4];
      unsigned px[4], hs[2][4], e[4], f0, f1;
      PV2("s_waitcnt lgkmcnt(5)\n\t", "=&v", "0", "0", 0, 0);
      PV2("s_waitcnt lgkmcnt(4)\n\t", "+v", "%0", "%1", 0, 1);
      PV2("s_waitcnt lgkmcnt(3)\n\t", "+v", "%0", "%1", 0, 2);
      PV2("s_waitcnt lgkmcnt(2)\n\t", "+v", "%0", "%1", 0, 3);
      asm volatile("s_waitcnt lgkmcnt(0)" : "+v"(s0), "+v"(s1) :: "memory");
      PV2("", "=&v", "0", "0", 2, 0);
      SB;
      s0 += s1;
      asm volatile("" : "+v"(s0));
      SB; PV1(2, 1); SB;
      f32x2 l2 = f32x2{s0[0], s0[1]} + f32x2{s0[2], s0[3]};
      asm volatile("" : "+v"(l2));
      SB; PV1(3, 1); SB;
      float lq = l2[0] + l2[1];
      asm volatile("" : "+v"(lq), "+v"(acc[0]), "+v"(acc[1]));
      SB;
#pragma unroll
      for (int k = 0; k < 4; ++k) px[k] = pack2(acc[0][k], acc[1][k]);
      asm volatile("" : "+v"(px[0]), "+v"(px[1]), "+v"(px[2]), "+v"(px[3]));
      SB; PV1(2, 2); SB;
      lq += dppf<0xB1>(lq);
      asm volatile("" : "+v"(lq));
      SB; PV1(3, 2); PV1(2, 3); SB;
      lq += dppf<0x4E>(lq);
      asm volatile("" : "+v"(lq));
      SB; PV1(3, 3);
      asm volatile(UD0(0, 10, 14, 16) UD0(1, 11, 14, 16) UD0(2, 12, 14, 16) UD0(3, 13, 14, 16) UD0(4, 10, 15, 17) UD0(5, 11, 15, 17) UD0(6, 12, 15, 17) UD0(7, 13, 15, 17)
                   : "=&v"(hs[0][0]), "=&v"(hs[0][1]), "=&v"(hs[0][2]), "=&v"(hs[0][3]), "=&v"(hs[1][0]), "=&v"(hs[1][1]), "=&v"(hs[1][2]), "=&v"(hs[1][3]), "+v"(acc[2]), "+v"(acc[3])
                   : "v"(px[0]), "v"(px[1]), "v"(px[2]), "v"(px[3]), "v"(hk[0].x), "v"(hk[1].x), "v"(hc[0]), "v"(hc[1]));
      SB;
#pragma unroll
      for (int k = 0; k < 4; ++k) {
        pw[k] = v2u{px[k], pack2(acc[2][k], acc[3][k])};
        __builtin_amdgcn_raw_buffer_store_b64(pw[k], rsw, o_pv, k == 0 ? 0 : k == 1 ? so1 : k == 2 ? so2 : so3, 16);
      }
      SB;
#define URO(o, i, bm) "v_add_u32_dpp %" #o ", %" #i ", %" #i " row_ror:8 row_mask:0xf bank_mask:" #bm "\n\t"
      asm volatile(UD(6, 14, 18) UD(10, 14, 19) UD(7, 15, 18) UD(11, 15, 19) UD(8, 16, 18) UD(12, 16, 19) UD(9, 17, 18) UD(13, 17, 19)
                   URO(2, 6, 0x3) URO(4, 10, 0x3) URO(3, 7, 0x3) URO(5, 11, 0x3) URO(2, 8, 0xc) URO(4, 12, 0xc) URO(3, 9, 0xc) URO(5, 13, 0xc)
                   UHM(0, 2, 0x5) UHM(1, 4, 0x5) UHM(0, 3, 0xa) UHM(1, 5, 0xa) "s_nop 0\n\t" UQA(0, "[1,0,3,2]") UQA(1, "[1,0,3,2]") "s_nop 0\n\t" UQA(0, "[2,3,0,1]") UQA(1, "[2,3,0,1]")
                   : "=&v"(f0), "=&v"(f1), "=&v"(e[0]), "=&v"(e[1]), "=&v"(e[2]), "=&v"(e[3]),
                     "+v"(hs[0][0]), "+v"(hs[0][1]), "+v"(hs[0][2]), "+v"(hs[0][3]), "+v"(hs[1][0]), "+v"(hs[1][1]), "+v"(hs[1][2]), "+v"(hs[1][3])
                   : "v"(pw[0].y), "v"(pw[1].y), "v"(pw[2].y), "v"(pw[3].y), "v"(hk[0].y), "v"(hk[1].y));
      __builtin_amdgcn_raw_buffer_store_b128(v4u{f0 + asu(lq), f1, asu(lq), tg}, rsw, o_ck, 0, 16);
      __builtin_amdgcn_s_setprio(0);
    }
    if (ffn_p) {
      asm volatile("s_waitcnt vmcnt(5)" : "+v"(wg[0]), "+v"(wg[1]), "+v"(wu[0]), "+v"(wu[1]), "+v"(xf[0]), "+v"(xf[1]) :: "memory");
      ffn_part();
    }
    if (has_ffn) {
      const unsigned dofs = ((fs * 16 + (lane >> 3) * 2) * MD + w * 64 + (lane & 7) * 8) * 2;
      v4u d0 = aload(w_down, dofs), d1 = aload(w_down + MD, dofs), xa = aload(x, lane * 16), ga = aload(gate_w, lane * 16);
      v4u fp[2] = {v4u{0u, 0u, 0u, 0u}, v4u{0u, 0u, 0u, 0u}}, fd[2];
      const unsigned fo = (unsigned)(WS_FP + ((lane & 7) * 688 + fs * 8 + (lane >> 3)) * 16);
      if (PRE_F) __builtin_amdgcn_s_sleep(PRE_F);
      u64 m = ~0ull;
      unsigned n = 0;
      for (;;) {
        poll2(fp[0], fo, m, fp[1], fo + 8 * 688 * 16, m, ws);
        m = stale(chk(T, chk(T, 0u, fp[0], fd[0]), fp[1], fd[1]));
        if (m == 0ull || ++n > SPIN_CAP) break;
        __builtin_amdgcn_s_sleep(SLPC);
      }
      asm volatile("" : "+v"(d0), "+v"(d1), "+v"(xa), "+v"(ga) :: "memory");
      f32x4 s4 = pay(fd[0]) + pay(fd[1]);
#pragma unroll
      for (int k = 0; k < 4; ++k) { s4[k] += dppf<0xB1>(s4[k]); s4[k] += dppf<0x4E>(s4[k]); s4[k] += dppf<0x141>(s4[k]); }
      const float og = 1.f - gate_of(xa, ga);
      const float h0 = s4[0] / (1.f + __expf(-s4[0])) * s4[1] * og, h1 = s4[2] / (1.f + __expf(-s4[2])) * s4[3] * og;
      float f0[8], f1[8];
      unpack8(d0, f0); unpack8(d1, f1);
      float b4[4];
#pragma unroll
      for (int k = 0; k < 4; ++k) {
        float v = sw32(h0 * f0[k] + h1 * f1[k], h0 * f0[k + 4] + h1 * f1[k + 4]);
        v = sw16(v, v);
        b4[k] = v + dppf<0x128>(v);
      }
      if ((lane & 24) == 0)
        __builtin_amdgcn_raw_buffer_store_b128(enc(T, b4[0], b4[1], b4[2], b4[3]), rsw, (int)(WS_FIN + (128 + fs) * 2048 + (w * 16 + (lane & 7) * 2 + (lane >> 5)) * 16), 0, 16);
    }
    if (w == 7) {
      const int k = xcd * 16 + ai;
      const unsigned fo = (unsigned)(WS_FIN + lane * 2048 + k * 16);
      v4u fv[4], fd[4];
      fv[0] = fv[1] = fv[2] = v4u{0u, 0u, 0u, 0u};
      fv[3] = v4u{T.t0, T.t1, T.t2, T.t3};
      unsigned n = 0;
      for (;;) {
        poll2(fv[0], (unsigned)(WS_ANN + lane * 128), ~0ull, fv[1], (unsigned)(WS_ANN + 8192 + lane * 128), ~0ull, ws);
        if (stale(chk(T, chk(T, 0u, fv[0], fd[0]), fv[1], fd[1])) == 0ull || ++n > SPIN_CAP) break;
        __builtin_amdgcn_s_sleep(4);
      }
      const unsigned tn = now();
      int dlt = max((int)(fv[0].x - (tn << 12)) >> 12, (int)(fv[1].x - (tn << 12)) >> 12);
      dlt = max(dlt, (int)dppu<0xB1>((unsigned)dlt)); dlt = max(dlt, (int)dppu<0x4E>((unsigned)dlt)); dlt = max(dlt, (int)dppu<0x141>((unsigned)dlt));
      dlt = max(dlt, (int)dppu<0x128>((unsigned)dlt));
      dlt = max(max(__builtin_amdgcn_readlane(dlt, 0), __builtin_amdgcn_readlane(dlt, 16)), max(__builtin_amdgcn_readlane(dlt, 32), __builtin_amdgcn_readlane(dlt, 48)));
      const unsigned tc = tn + dlt + DC_;
      wait_until(tc - 80);
      u64 m = ~0ull;
      n = 0;
      for (;;) {
        poll2(fv[2], fo + 262144, m, fv[3], fo + 393216, m & ((1ull << (NFS - 64)) - 1), ws);
        m = stale(chk(T, chk(T, 0u, fv[2], fd[2]), fv[3], fd[3]));
        if (m == 0ull || ++n > SPIN_CAP) break;
        __builtin_amdgcn_s_sleep(SLPC);
      }
      wait_until(tc);
      m = ~0ull;
      n = 0;
      for (;;) {
        poll2(fv[0], fo, m, fv[1], fo + 131072, m, ws);
        m = stale(chk(T, chk(T, 0u, fv[0], fd[0]), fv[1], fd[1]));
        if (m == 0ull || ++n > SPIN_CAP) break;
        __builtin_amdgcn_s_sleep(SLPC);
      }
      const f32x4 s4 = (pay(fd[0]) + pay(fd[1])) + (pay(fd[2]) + pay(fd[3]));
      const float v = row_sum(sw16(sw32(s4[0], s4[2]), sw32(s4[1], s4[3])));
      asm volatile(".Lcpf_end:" ::: "memory");
      if ((lane & 15) == 0) out[k * 4 + (lane >> 4)] = f2bf(v);
    }
  }
}

extern "C" void kernel_launch(void* const* d_in, const int* in_sizes, int n_in, void* d_out, int out_size,
                              void* d_ws, size_t ws_size, hipStream_t stream) {
  const u16* x = (const u16*)d_in[0];
  const u16* c_kv = (const u16*)d_in[1];
  const u16* w_q = (const u16*)d_in[2];
  const u16* w_uk = (const u16*)d_in[3];
  const u16* w_uv = (const u16*)d_in[4];
  const u16* w_o = (const u16*)d_in[5];
  const u16* w_gate = (const u16*)d_in[7];
  const u16* w_up = (const u16*)d_in[8];
  const u16* w_down = (const u16*)d_in[9];
  const u16* gate_w = (const u16*)d_in[10];
  unsigned char* ws = (unsigned char*)d_ws;
  k3_mix<<<NWG, NT, 0, stream>>>(x, c_kv, w_q, w_gate, w_up, w_uk, w_uv, w_o, w_down, gate_w, ws, (u16*)d_out);
}
```
